```python
import torch

def _softmax(x, axis=-1):
    return torch.softmax(x, dim=axis)

def _gelu(x):
    return torch.nn.functional.gelu(x, approximate='tanh')

def _silu(x):
    return torch.nn.functional.silu(x)

def _sigmoid(x):
    return torch.sigmoid(x)

def _layernorm(x, axis=-1, eps=1e-05):
    m = x.mean(dim=axis, keepdim=True)
    v = x.var(dim=axis, keepdim=True, unbiased=False)
    return (x - m) / torch.sqrt(v + eps)

def _scatter_add(tgt, idx, src, axis=0):
    return tgt.clone().index_add_(axis, idx, src)

def _segment_sum(src, seg, n):
    out = torch.zeros((n,) + src.shape[1:], dtype=src.dtype)
    return out.index_add_(0, seg, src)

def _pad(x, pw):
    flat = tuple((v for pair in reversed(pw) for v in pair))
    return torch.nn.functional.pad(x, flat)
kernel_name = 'standalone__kv_cache_quant_pre_rope_vs_post_rope__decode'

def setup_inputs():

    _w = lambda a: (lambda t: t.to(torch.bfloat16) if t.is_floating_point() else t)(torch.as_tensor(a))
    B, Sq, Skv = (2, 1, 4096)
    H, Dh0 = (32, 192)
    G = 128 if 128 > 0 else Dh0
    G = max(2, G // 2 * 2)
    Dh = max(G, Dh0 // G * G)
    Skv = max(Skv, Sq)
    ng = Dh // G
    bits = 8 if 8 > 1 else 4
    bits = max(2, min(8, bits))
    qmax = (1 << bits) - 1
    half = Dh // 2
    inv = 1.0 / 10000.0 ** (torch.arange(half) / max(1, half))
    ang_k = torch.arange(Skv)[:, None] * inv[None, :]
    ang_q = torch.arange(Skv - Sq, Skv)[:, None] * inv[None, :]
    kc = torch.cat([torch.cos(ang_k), torch.cos(ang_k)], dim=-1)
    ks = torch.cat([torch.sin(ang_k), torch.sin(ang_k)], dim=-1)
    qc = torch.cat([torch.cos(ang_q), torch.cos(ang_q)], dim=-1)
    qs = torch.cat([torch.sin(ang_q), torch.sin(ang_q)], dim=-1)
    qk_i = torch.randint(0, qmax + 1, (B, H, Skv, Dh)).to(torch.int32)
    qv_i = torch.randint(0, qmax + 1, (B, H, Skv, Dh)).to(torch.int32)
    ksc = torch.abs((lambda *s: torch.randn(*s, dtype=torch.bfloat16) * 0.05)(B, H, Skv, ng)) * 0.1 + 0.01
    kzp = torch.full((B, H, Skv, ng), qmax / 2.0)
    vsc = torch.abs((lambda *s: torch.randn(*s, dtype=torch.bfloat16) * 0.05)(B, H, Skv, ng)) * 0.1 + 0.01
    vzp = torch.full((B, H, Skv, ng), qmax / 2.0)
    return {'q': _w((lambda *s: torch.randn(*s, dtype=torch.bfloat16) * 0.8)(B, H, Sq, Dh) * 0.5), 'qk': _w(qk_i), 'k_scales': _w(ksc), 'k_zeros': _w(kzp), 'qv': _w(qv_i), 'v_scales': _w(vsc), 'v_zeros': _w(vzp), 'q_cos': _w(qc), 'q_sin': _w(qs), 'k_cos': _w(kc), 'k_sin': _w(ks)}

def reference(q, qk, k_scales, k_zeros, qv, v_scales, v_zeros, q_cos, q_sin, k_cos, k_sin):
    B, H, Sq, Dh = q.shape
    ng = k_scales.shape[-1]
    G = Dh // ng
    half = Dh // 2
    ks = (lambda x, n, axis: torch.repeat_interleave(x, n, dim=axis))(k_scales, G, axis=-1)
    kz = (lambda x, n, axis: torch.repeat_interleave(x, n, dim=axis))(k_zeros, G, axis=-1)
    k = ((lambda x, dt: x.to(dt))(qk, q.dtype) - kz) * ks
    vs = (lambda x, n, axis: torch.repeat_interleave(x, n, dim=axis))(v_scales, G, axis=-1)
    vz = (lambda x, n, axis: torch.repeat_interleave(x, n, dim=axis))(v_zeros, G, axis=-1)
    v = ((lambda x, dt: x.to(dt))(qv, q.dtype) - vz) * vs
    k1 = k[..., :half].clone()
    k2 = k[..., half:].clone()
    krh = torch.concatenate([-k2, k1], axis=-1)
    kr = k * k_cos[None, None, :, :] + krh * k_sin[None, None, :, :]
    q1 = q[..., :half].clone()
    q2 = q[..., half:].clone()
    qrh = torch.concatenate([-q2, q1], axis=-1)
    qr = q * q_cos[None, None, :, :] + qrh * q_sin[None, None, :, :]
    logits = torch.einsum('bhqd,bhkd->bhqk', qr, kr) * float(Dh) ** (-0.5)
    attn = _softmax(logits, axis=-1)
    out = torch.einsum('bhqk,bhkd->bhqd', attn, v)
    return out
```

```cpp
#include <hip/hip_runtime.h>
#include <hip/hip_bf16.h>
#include <stdint.h>

namespace {

constexpr int B_ = 2, H_ = 32, SKV = 4096, D = 128;
constexpr int BH = B_ * H_;
constexpr int NSPLIT = 8;
constexpr int WAVES = 4;
constexpr int NTHR = WAVES * 64;
constexpr int TOK_WG = SKV / NSPLIT;
constexpr int TOK_WAVE = TOK_WG / WAVES;
constexpr int UNIT = 8;
constexpr int NUNIT = TOK_WAVE / UNIT;
constexpr float SCALE_LOG2E = 0.088388347648318440f * 1.4426950408889634f;

using u32x4 = __attribute__((ext_vector_type(4))) unsigned;
using u32x2 = __attribute__((ext_vector_type(2))) unsigned;
using i32x4 = __attribute__((ext_vector_type(4))) int;
using f32x4 = __attribute__((ext_vector_type(4))) float;
using bf16x2 = __attribute__((ext_vector_type(2))) __bf16;

__device__ __forceinline__ float bf2f(unsigned short h) { return __uint_as_float(((unsigned)h) << 16); }
__device__ __forceinline__ float rbf(float x) {
  return (float)(__bf16)x;
}
__device__ __forceinline__ unsigned packbf(float lo, float hi) {
  bf16x2 v; v[0] = (__bf16)lo; v[1] = (__bf16)hi;
  return __builtin_bit_cast(unsigned, v);
}
__device__ __forceinline__ float dot2(unsigned a, unsigned b, float acc) {
  return __builtin_amdgcn_fdot2_f32_bf16(__builtin_bit_cast(bf16x2, a), __builtin_bit_cast(bf16x2, b), acc, false);
}
template <int CTRL> __device__ __forceinline__ float dpp(float x) {
  return __builtin_bit_cast(float, __builtin_amdgcn_mov_dpp(__builtin_bit_cast(int, x), CTRL, 0xf, 0xf, true));
}
constexpr int XOR1 = 0xB1, XOR2 = 0x4E, ROR4 = 0x124, ROR8 = 0x128;
__device__ __forceinline__ float x16_sum(float x) {
  auto s = __builtin_amdgcn_permlane16_swap(__float_as_uint(x), __float_as_uint(x), false, false);
  return __uint_as_float(s[0]) + __uint_as_float(s[1]);
}
__device__ __forceinline__ float x32_sum(float x) {
  auto s = __builtin_amdgcn_permlane32_swap(__float_as_uint(x), __float_as_uint(x), false, false);
  return __uint_as_float(s[0]) + __uint_as_float(s[1]);
}
__device__ __forceinline__ float x32_max(float x) {
  auto s = __builtin_amdgcn_permlane32_swap(__float_as_uint(x), __float_as_uint(x), false, false);
  return fmaxf(__uint_as_float(s[0]), __uint_as_float(s[1]));
}

struct Args {
  const unsigned short* q;
  const int* qk;
  const unsigned short* ksc;
  const unsigned short* kzp;
  const int* qv;
  const unsigned short* vsc;
  const unsigned short* vzp;
  const unsigned short* qcos;
  const unsigned short* qsin;
  const unsigned short* kcos;
  const unsigned short* ksin;
  float* ws;
};

struct Unit {
  i32x4 k[4];
  i32x4 v[4];
  u32x2 c[4];
  u32x2 s[4];
};

constexpr int REC = 132;

__global__ void __launch_bounds__(NTHR) attn_split(Args a) {
  __shared__ __attribute__((aligned(16))) float lds[TOK_WG * 4 + WAVES * REC];
  f32x4* sc4 = reinterpret_cast<f32x4*>(lds);
  float* mrg = lds + TOK_WG * 4;

  const int split = blockIdx.x % NSPLIT;
  const int bh = blockIdx.x / NSPLIT;
  const int tid = threadIdx.x;
  const int lane = tid & 63;
  const int wave = __builtin_amdgcn_readfirstlane(tid >> 6);
  const int r = lane >> 5, c = lane & 31;
  const int tw0 = split * TOK_WG;
  const int t0 = tw0 + wave * TOK_WAVE;

  const char* kbase = reinterpret_cast<const char*>(a.qk) + ((size_t)bh * SKV + t0) * (D * 4);
  const char* vbase = reinterpret_cast<const char*>(a.qv) + ((size_t)bh * SKV + t0) * (D * 4);
  const char* cbase = reinterpret_cast<const char*>(a.kcos) + (size_t)t0 * (D * 2);
  const char* sbase = reinterpret_cast<const char*>(a.ksin) + (size_t)t0 * (D * 2);
  const unsigned loff16 = lane * 16;
  const unsigned loffc = lane * 8;
  const unsigned loffs = (lane * 8) ^ 128;

  auto issue = [&](Unit& U, int u) {
#pragma unroll
    for (int i = 0; i < 4; ++i)
      U.k[i] = __builtin_nontemporal_load(reinterpret_cast<const i32x4*>(kbase + (size_t)u * 4096 + i * 1024 + loff16));
#pragma unroll
    for (int i = 0; i < 4; ++i) {
      U.c[i] = *reinterpret_cast<const u32x2*>(cbase + (size_t)u * 2048 + i * 512 + loffc);
      U.s[i] = *reinterpret_cast<const u32x2*>(sbase + (size_t)u * 2048 + i * 512 + loffs);
    }
#pragma unroll
    for (int i = 0; i < 4; ++i)
      U.v[i] = __builtin_nontemporal_load(reinterpret_cast<const i32x4*>(vbase + (size_t)u * 4096 + i * 1024 + loff16));
  };

  const int d0 = 4 * c, d1 = (4 * c + 64) & 127;
  const u32x2 q0 = *reinterpret_cast<const u32x2*>(a.q + bh * D + d0);
  const u32x2 q1 = *reinterpret_cast<const u32x2*>(a.q + bh * D + d1);
  const u32x2 c0 = *reinterpret_cast<const u32x2*>(a.qcos + d0);
  const u32x2 c1 = *reinterpret_cast<const u32x2*>(a.qcos + d1);
  const u32x2 s0 = *reinterpret_cast<const u32x2*>(a.qsin + d0);
  const u32x2 s1 = *reinterpret_cast<const u32x2*>(a.qsin + d1);
  const size_t sidx = (size_t)bh * SKV + tw0 + 2 * tid;
  const unsigned ks2 = *reinterpret_cast<const unsigned*>(a.ksc + sidx);
  const unsigned kz2 = *reinterpret_cast<const unsigned*>(a.kzp + sidx);
  const unsigned vs2 = *reinterpret_cast<const unsigned*>(a.vsc + sidx);
  const unsigned vz2 = *reinterpret_cast<const unsigned*>(a.vzp + sidx);

  Unit UA, UB;
  issue(UA, 0);
  issue(UB, 1);

  {
    f32x4 a0 = {__uint_as_float(ks2 << 16) * SCALE_LOG2E, __uint_as_float(kz2 << 16), __uint_as_float(vs2 << 16), __uint_as_float(vz2 << 16)};
    f32x4 a1 = {__uint_as_float(ks2 & 0xffff0000u) * SCALE_LOG2E, __uint_as_float(kz2 & 0xffff0000u), __uint_as_float(vs2 & 0xffff0000u), __uint_as_float(vz2 & 0xffff0000u)};
    sc4[2 * tid] = a0;
    sc4[2 * tid + 1] = a1;
  }

  unsigned qab[4];
  {
    const bool lo = d0 < 64;
#pragma unroll
    for (int j = 0; j < 4; ++j) {
      auto ex = [&](u32x2 w) { unsigned x = w[j >> 1]; return __uint_as_float((j & 1) ? (x & 0xffff0000u) : (x << 16)); };
      const float qa_ = ex(q0), qb_ = ex(q1), ca = ex(c0), cb = ex(c1), sa = ex(s0), sb = ex(s1);
      const float ra = rbf(rbf(qa_ * ca) + rbf((lo ? -qb_ : qb_) * sa));
      const float rb = rbf(rbf(qb_ * cb) + rbf((lo ? qa_ : -qa_) * sb));
      qab[j] = packbf(ra, lo ? rb : -rb);
    }
  }
  __syncthreads();

  float m = -INFINITY, l = 0.f, corr = 0.f;
  float acc[4] = {0.f, 0.f, 0.f, 0.f};
  const int cls = c & 3;
  const bool b0 = c & 1, b1 = c & 2;

  auto compute = [&](const Unit& U, int u) {
    const f32x4 sc = sc4[wave * TOK_WAVE + u * UNIT + 2 * cls + r];
    float x[4];
#pragma unroll
    for (int i = 0; i < 4; ++i) {
      const float kz = i == 0 ? dpp<0x00>(sc[1]) : i == 1 ? dpp<0x55>(sc[1]) : i == 2 ? dpp<0xAA>(sc[1]) : dpp<0xFF>(sc[1]);
      float d = 0.f;
#pragma unroll
      for (int j = 0; j < 4; ++j) {
        const unsigned cw = U.c[i][j >> 1], sw = U.s[i][j >> 1];
        const unsigned cs = (j & 1) ? __builtin_amdgcn_perm(sw, cw, 0x07060302u) : __builtin_amdgcn_perm(sw, cw, 0x05040100u);
        const float w = dot2(cs, qab[j], 0.f);
        d = fmaf((float)U.k[i][j] - kz, w, d);
      }
      x[i] = d;
    }
    const float e0 = x[0] + dpp<XOR1>(x[0]), e1 = x[1] + dpp<XOR1>(x[1]);
    const float e2 = x[2] + dpp<XOR1>(x[2]), e3 = x[3] + dpp<XOR1>(x[3]);
    const float y0 = b0 ? e1 : e0;
    const float y1 = b0 ? e3 : e2;
    const float g0 = y0 + dpp<XOR2>(y0), g1 = y1 + dpp<XOR2>(y1);
    float z = b1 ? g1 : g0;
    z += dpp<ROR4>(z);
    z += dpp<ROR8>(z);
    z = x16_sum(z);
    const float s = z * sc[0];
    float pm = fmaxf(s, dpp<XOR1>(s));
    pm = fmaxf(pm, dpp<XOR2>(pm));
    const float mn = fmaxf(m, pm);
    const float alpha = __builtin_amdgcn_exp2f(m - mn);
    const float p = __builtin_amdgcn_exp2f(s - mn);
    m = mn;
    l = fmaf(l, alpha, p);
    const float pv = p * sc[2];
    corr = fmaf(corr, alpha, pv * sc[3]);
#pragma unroll
    for (int j = 0; j < 4; ++j) acc[j] *= alpha;
#pragma unroll
    for (int i = 0; i < 4; ++i) {
      const float pi = i == 0 ? dpp<0x00>(pv) : i == 1 ? dpp<0x55>(pv) : i == 2 ? dpp<0xAA>(pv) : dpp<0xFF>(pv);
#pragma unroll
      for (int j = 0; j < 4; ++j) acc[j] = fmaf(pi, (float)U.v[i][j], acc[j]);
    }
  };

#pragma unroll 1
  for (int u = 0; u < NUNIT; u += 2) {
    compute(UA, u);
    if (u + 2 < NUNIT) issue(UA, u + 2);
    compute(UB, u + 1);
    if (u + 3 < NUNIT) issue(UB, u + 3);
  }

  l += dpp<XOR1>(l); l += dpp<XOR2>(l);
  corr += dpp<XOR1>(corr); corr += dpp<XOR2>(corr);
  {
    const float M = x32_max(m);
    const float f = __builtin_amdgcn_exp2f(m - M);
    l = x32_sum(l * f);
#pragma unroll
    for (int j = 0; j < 4; ++j) acc[j] = x32_sum((acc[j] - corr) * f);
    m = M;
  }
  if (r == 0) {
    float* rec = mrg + wave * REC;
    *reinterpret_cast<f32x4*>(rec + 4 + 4 * c) = f32x4{acc[0], acc[1], acc[2], acc[3]};
    if (c == 0) { rec[0] = m; rec[1] = l; }
  }
  __syncthreads();
  if (tid < D) {
    float M = -INFINITY;
#pragma unroll
    for (int w = 0; w < WAVES; ++w) M = fmaxf(M, mrg[w * REC]);
    float L = 0.f, o = 0.f;
#pragma unroll
    for (int w = 0; w < WAVES; ++w) {
      const float f = __builtin_amdgcn_exp2f(mrg[w * REC] - M);
      L = fmaf(f, mrg[w * REC + 1], L);
      o = fmaf(f, mrg[w * REC + 4 + tid], o);
    }
    float* prec = a.ws + ((size_t)bh * NSPLIT + split) * REC;
    prec[4 + tid] = o;
    if (tid == 0) { prec[0] = M; prec[1] = L; }
  }
}

__global__ void __launch_bounds__(D) attn_combine(const float* ws, __hip_bfloat16* out) {
  const int bh = blockIdx.x, d = threadIdx.x;
  const float* p = ws + (size_t)bh * NSPLIT * REC;
  float M = -INFINITY;
#pragma unroll
  for (int s = 0; s < NSPLIT; ++s) M = fmaxf(M, p[s * REC]);
  float L = 0.f, o = 0.f;
#pragma unroll
  for (int s = 0; s < NSPLIT; ++s) {
    const float f = __builtin_amdgcn_exp2f(p[s * REC] - M);
    L = fmaf(f, p[s * REC + 1], L);
    o = fmaf(f, p[s * REC + 4 + d], o);
  }
  out[bh * D + d] = __float2bfloat16(o / L);
}

}

extern "C" void kernel_launch(void* const* d_in, const int* in_sizes, int n_in,
                              void* d_out, int out_size, void* d_ws, size_t ws_size,
                              hipStream_t stream) {
  Args a;
  a.q = (const unsigned short*)d_in[0];
  a.qk = (const int*)d_in[1];
  a.ksc = (const unsigned short*)d_in[2];
  a.kzp = (const unsigned short*)d_in[3];
  a.qv = (const int*)d_in[4];
  a.vsc = (const unsigned short*)d_in[5];
  a.vzp = (const unsigned short*)d_in[6];
  a.qcos = (const unsigned short*)d_in[7];
  a.qsin = (const unsigned short*)d_in[8];
  a.kcos = (const unsigned short*)d_in[9];
  a.ksin = (const unsigned short*)d_in[10];
  a.ws = (float*)d_ws;
  attn_split<<<BH * NSPLIT, NTHR, 0, stream>>>(a);
  attn_combine<<<BH, D, 0, stream>>>((const float*)d_ws, (__hip_bfloat16*)d_out);
}
```

```cpp
#include <hip/hip_runtime.h>
#include <hip/hip_bf16.h>
#include <stdint.h>

#define WAVES_ 4
#define NBUF 2
#define TABAUX 2
#define PROBAR 1
#define DYN 1

namespace {

constexpr int B_ = 2, H_ = 32, SKV = 4096, D = 128;
constexpr int BH = B_ * H_;
constexpr int HG = 4;
constexpr int NGRP = BH / HG;
constexpr int NWG = 256;
constexpr int NSPLIT = NWG / NGRP;
constexpr int WAVES = WAVES_;
constexpr int NTHR = WAVES * 64;
constexpr int TOK_WG = SKV / NSPLIT;
constexpr int NUNIT = TOK_WG / 2 / WAVES;
constexpr float SCALE_LOG2E = 0.088388347648318440f * 1.4426950408889634f;
constexpr unsigned HSTRIDE = (unsigned)SKV * D * 4;

constexpr unsigned WS_EPOCH = 0;
constexpr unsigned WS_ML = 4096;
constexpr unsigned WS_ACC = WS_ML + NGRP * HG * NSPLIT * 16;
constexpr unsigned WS_END = WS_ACC + NGRP * HG * 4 * NSPLIT * 256;

using u32x4 = __attribute__((ext_vector_type(4))) unsigned;
using u32x2 = __attribute__((ext_vector_type(2))) unsigned;
using f32x4 = __attribute__((ext_vector_type(4))) float;
using f32x2 = __attribute__((ext_vector_type(2))) float;
using bf16x2 = __attribute__((ext_vector_type(2))) __bf16;

__device__ __forceinline__ float rbf(float x) { return (float)(__bf16)x; }
__device__ __forceinline__ float bfu(unsigned short h) { return __uint_as_float((unsigned)h << 16); }
__device__ __forceinline__ unsigned packbf(float lo, float hi) {
  bf16x2 v; v[0] = (__bf16)lo; v[1] = (__bf16)hi;
  return __builtin_bit_cast(unsigned, v);
}
__device__ __forceinline__ float dot2(unsigned a, unsigned b, float acc) {
  return __builtin_amdgcn_fdot2_f32_bf16(__builtin_bit_cast(bf16x2, a), __builtin_bit_cast(bf16x2, b), acc, false);
}
template <int CTRL> __device__ __forceinline__ float dpp(float x) {
  return __builtin_bit_cast(float, __builtin_amdgcn_mov_dpp(__builtin_bit_cast(int, x), CTRL, 0xf, 0xf, true));
}
template <int I> __device__ __forceinline__ float qbc(float x) { return dpp<I * 0x55>(x); }
constexpr int XOR1 = 0xB1, XOR2 = 0x4E, ROR4 = 0x124, ROR8 = 0x128;
__device__ __forceinline__ float x16_sum(float x) {
  auto s = __builtin_amdgcn_permlane16_swap(__float_as_uint(x), __float_as_uint(x), false, false);
  return __uint_as_float(s[0]) + __uint_as_float(s[1]);
}
__device__ __forceinline__ float x16_max(float x) {
  auto s = __builtin_amdgcn_permlane16_swap(__float_as_uint(x), __float_as_uint(x), false, false);
  return fmaxf(__uint_as_float(s[0]), __uint_as_float(s[1]));
}
__device__ __forceinline__ float x32_sum(float x) {
  auto s = __builtin_amdgcn_permlane32_swap(__float_as_uint(x), __float_as_uint(x), false, false);
  return __uint_as_float(s[0]) + __uint_as_float(s[1]);
}
__device__ __forceinline__ float x32_max(float x) {
  auto s = __builtin_amdgcn_permlane32_swap(__float_as_uint(x), __float_as_uint(x), false, false);
  return fmaxf(__uint_as_float(s[0]), __uint_as_float(s[1]));
}

struct Args {
  const unsigned short* q;
  const int* qk;
  const unsigned short* ksc;
  const unsigned short* kzp;
  const int* qv;
  const unsigned short* vsc;
  const unsigned short* vzp;
  const unsigned short* qcos;
  const unsigned short* qsin;
  const unsigned short* kcos;
  const unsigned short* ksin;
  unsigned char* ws;
  __hip_bfloat16* out;
};

struct Unit {
  u32x4 k[HG];
  u32x4 v[HG];
  u32x2 c;
  u32x2 s;
};

constexpr int REC = 132;
constexpr int LDS_SC = 0;
constexpr int LDS_QAB = TOK_WG * HG * 4;
constexpr int LDS_MRG = LDS_QAB + HG * D;
constexpr int LDS_TICK = LDS_MRG + WAVES * HG * REC;
constexpr int LDS_FLOATS = 21 * 1024;
static_assert(LDS_TICK + 4 <= LDS_FLOATS, "");
constexpr int NUWG = TOK_WG / 2;
constexpr unsigned UOOB = 1u << 21;

__global__ void __launch_bounds__(NTHR) attn_decode(Args a) {
  __shared__ __attribute__((aligned(16))) float lds[LDS_FLOATS];
  f32x4* sc4 = reinterpret_cast<f32x4*>(lds + LDS_SC);
  unsigned* qabl = reinterpret_cast<unsigned*>(lds + LDS_QAB);
  float* mrg = lds + LDS_MRG;
  unsigned* tick = reinterpret_cast<unsigned*>(lds + LDS_TICK);
  const int split = blockIdx.x % NSPLIT;
  const int grp = blockIdx.x / NSPLIT;
  const int tid = threadIdx.x;
  const int lane = tid & 63;
  const int wave = __builtin_amdgcn_readfirstlane(tid >> 6);
  const int r = lane >> 5, c = lane & 31;
  const int cls = c & 3;
  const int bh0 = grp * HG;
  const int tw0 = split * TOK_WG;
  const int t0 = tw0;

  const auto krs = __builtin_amdgcn_make_buffer_rsrc((void*)(reinterpret_cast<const char*>(a.qk) + (size_t)bh0 * HSTRIDE), 0, HG * HSTRIDE, 0x00020000);
  const auto vrs = __builtin_amdgcn_make_buffer_rsrc((void*)(reinterpret_cast<const char*>(a.qv) + (size_t)bh0 * HSTRIDE), 0, HG * HSTRIDE, 0x00020000);
  const auto crs = __builtin_amdgcn_make_buffer_rsrc((void*)a.kcos, 0, SKV * D * 2, 0x00020000);
  const auto srs = __builtin_amdgcn_make_buffer_rsrc((void*)a.ksin, 0, SKV * D * 2, 0x00020000);
  const auto wrs = __builtin_amdgcn_make_buffer_rsrc((void*)a.ws, 0, WS_END, 0x00020000);
  const unsigned koff = (unsigned)t0 * (D * 4) + lane * 16;
  const unsigned coff = (unsigned)t0 * (D * 2) + lane * 8;
  const unsigned soff = (unsigned)t0 * (D * 2) + ((lane * 8) ^ 128);

  auto issue = [&](Unit& U, unsigned uu) {
    U.c = __builtin_amdgcn_raw_buffer_load_b64(crs, coff + uu * 512, 0, TABAUX);
    U.s = __builtin_amdgcn_raw_buffer_load_b64(srs, soff + uu * 512, 0, TABAUX);
#pragma unroll
    for (int i = 0; i < HG; ++i) U.k[i] = __builtin_amdgcn_raw_buffer_load_b128(krs, koff + uu * 1024, i * HSTRIDE, 2);
#pragma unroll
    for (int i = 0; i < HG; ++i) U.v[i] = __builtin_amdgcn_raw_buffer_load_b128(vrs, koff + uu * 1024, i * HSTRIDE, 2);
  };

  const int qh = wave & (HG - 1);
  const unsigned short* qp = a.q + (bh0 + qh) * D;
  const unsigned short q_lo = qp[lane], q_hi = qp[lane + 64];
  const unsigned short c_lo = a.qcos[lane], c_hi = a.qcos[lane + 64];
  const unsigned short s_lo = a.qsin[lane], s_hi = a.qsin[lane + 64];
  constexpr int NITEM = HG * TOK_WG / 2 / NTHR;
  unsigned sraw[NITEM][4];
#pragma unroll
  for (int j = 0; j < NITEM; ++j) {
    const int it = tid + NTHR * j;
    const int hh = it / (TOK_WG / 2), pp = it % (TOK_WG / 2);
    const size_t sidx = (size_t)(bh0 + hh) * SKV + tw0 + 2 * pp;
    sraw[j][0] = __builtin_nontemporal_load(reinterpret_cast<const unsigned*>(a.ksc + sidx));
    sraw[j][1] = __builtin_nontemporal_load(reinterpret_cast<const unsigned*>(a.kzp + sidx));
    sraw[j][2] = __builtin_nontemporal_load(reinterpret_cast<const unsigned*>(a.vsc + sidx));
    sraw[j][3] = __builtin_nontemporal_load(reinterpret_cast<const unsigned*>(a.vzp + sidx));
  }
  const unsigned tag = __builtin_amdgcn_raw_buffer_load_b32(wrs, WS_EPOCH + grp * 256, 0, 16) + 1u;
  if (tid == 0) *tick = NBUF * WAVES;
  __builtin_amdgcn_sched_barrier(0);
  __builtin_amdgcn_s_barrier();
  __builtin_amdgcn_sched_barrier(0);

  Unit U[NBUF];
  unsigned uid[NBUF];
#pragma unroll
  for (int b = 0; b < NBUF; ++b) { uid[b] = b * WAVES + wave; issue(U[b], uid[b]); __builtin_amdgcn_sched_barrier(0); }

  {
    const float q1 = bfu(q_lo), q2 = bfu(q_hi);
    const float r1 = rbf(rbf(q1 * bfu(c_lo)) + rbf(-q2 * bfu(s_lo)));
    const float r2 = rbf(rbf(q2 * bfu(c_hi)) + rbf(q1 * bfu(s_hi)));
    if (wave < HG) {
      qabl[qh * D + lane] = packbf(r1, r2);
      qabl[qh * D + lane + 64] = packbf(r2, -r1);
    }
  }
#pragma unroll
  for (int j = 0; j < NITEM; ++j) {
    const int it = tid + NTHR * j;
    const int hh = it / (TOK_WG / 2), pp = it % (TOK_WG / 2);
    f32x4 a0 = {__uint_as_float(sraw[j][0] << 16) * SCALE_LOG2E, __uint_as_float(sraw[j][1] << 16), __uint_as_float(sraw[j][2] << 16), __uint_as_float(sraw[j][3] << 16)};
    f32x4 a1 = {__uint_as_float(sraw[j][0] & 0xffff0000u) * SCALE_LOG2E, __uint_as_float(sraw[j][1] & 0xffff0000u), __uint_as_float(sraw[j][2] & 0xffff0000u), __uint_as_float(sraw[j][3] & 0xffff0000u)};
    sc4[(2 * pp) * HG + hh] = a0;
    sc4[(2 * pp + 1) * HG + hh] = a1;
  }
  __syncthreads();
  u32x4 qab[HG];
#pragma unroll
  for (int i = 0; i < HG; ++i) qab[i] = *reinterpret_cast<const u32x4*>(qabl + i * D + 4 * c);

  float m = -INFINITY, l = 0.f, corr = 0.f;
  float acc[HG][4];
#pragma unroll
  for (int i = 0; i < HG; ++i)
#pragma unroll
    for (int j = 0; j < 4; ++j) acc[i][j] = 0.f;
  const bool b0 = c & 1, b1 = c & 2;

  auto compute = [&](const Unit& U, unsigned u) {
    const f32x4 sc = sc4[(2 * u + r) * HG + cls];
    unsigned cs[4];
    cs[0] = __builtin_amdgcn_perm(U.s[0], U.c[0], 0x05040100u);
    cs[1] = __builtin_amdgcn_perm(U.s[0], U.c[0], 0x07060302u);
    cs[2] = __builtin_amdgcn_perm(U.s[1], U.c[1], 0x05040100u);
    cs[3] = __builtin_amdgcn_perm(U.s[1], U.c[1], 0x07060302u);
    float x[HG];
#pragma unroll
    for (int i = 0; i < HG; ++i) {
      const float kz = i == 0 ? qbc<0>(sc[1]) : i == 1 ? qbc<1>(sc[1]) : i == 2 ? qbc<2>(sc[1]) : qbc<3>(sc[1]);
      float d = 0.f;
#pragma unroll
      for (int j = 0; j < 4; ++j) {
        const float w = dot2(cs[j], qab[i][j], 0.f);
        d = fmaf((float)(int)U.k[i][j] - kz, w, d);
      }
      x[i] = d;
    }
    const float e0 = x[0] + dpp<XOR1>(x[0]), e1 = x[1] + dpp<XOR1>(x[1]);
    const float e2 = x[2] + dpp<XOR1>(x[2]), e3 = x[3] + dpp<XOR1>(x[3]);
    const float y0 = b0 ? e1 : e0;
    const float y1 = b0 ? e3 : e2;
    const float g0 = y0 + dpp<XOR2>(y0), g1 = y1 + dpp<XOR2>(y1);
    float z = b1 ? g1 : g0;
    z += dpp<ROR4>(z);
    z += dpp<ROR8>(z);
    z = x16_sum(z);
    const float s = z * sc[0];
    const float mn = fmaxf(m, s);
    const float alpha = __builtin_amdgcn_exp2f(m - mn);
    const float p = __builtin_amdgcn_exp2f(s - mn);
    m = mn;
    l = fmaf(l, alpha, p);
    const float pv = p * sc[2];
    corr = fmaf(corr, alpha, pv * sc[3]);
#pragma unroll
    for (int i = 0; i < HG; ++i) {
      const float al = i == 0 ? qbc<0>(alpha) : i == 1 ? qbc<1>(alpha) : i == 2 ? qbc<2>(alpha) : qbc<3>(alpha);
      const float pi = i == 0 ? qbc<0>(pv) : i == 1 ? qbc<1>(pv) : i == 2 ? qbc<2>(pv) : qbc<3>(pv);
#pragma unroll
      for (int j = 0; j < 4; ++j) acc[i][j] = fmaf(pi, (float)(int)U.v[i][j], acc[i][j] * al);
    }
  };

  {
    bool more = true;
#pragma unroll 1
    for (int it = 0; more && it < NUWG; ++it) {
      more = false;
#pragma unroll
      for (int b = 0; b < NBUF; ++b) {
        if (uid[b] < (unsigned)NUWG) compute(U[b], uid[b]);
        __builtin_amdgcn_sched_barrier(0);
        unsigned t = 0;
        if (lane == 0) t = __hip_atomic_fetch_add(tick, 1u, __ATOMIC_RELAXED, __HIP_MEMORY_SCOPE_WORKGROUP);
        t = __builtin_amdgcn_readfirstlane(t);
        const bool valid = t < (unsigned)NUWG;
        uid[b] = valid ? t : UOOB;
        more = more || valid;
        issue(U[b], uid[b]);
        __builtin_amdgcn_sched_barrier(0);
      }
    }
  }

  {
    const float M = x32_max(m);
    const float f = __builtin_amdgcn_exp2f(m - M);
    l = x32_sum(l * f);
#pragma unroll
    for (int i = 0; i < HG; ++i) {
      const float fi = i == 0 ? qbc<0>(f) : i == 1 ? qbc<1>(f) : i == 2 ? qbc<2>(f) : qbc<3>(f);
      const float ci = i == 0 ? qbc<0>(corr) : i == 1 ? qbc<1>(corr) : i == 2 ? qbc<2>(corr) : qbc<3>(corr);
#pragma unroll
      for (int j = 0; j < 4; ++j) acc[i][j] = x32_sum((acc[i][j] - ci) * fi);
    }
    m = M;
  }
  if (r == 0) {
#pragma unroll
    for (int i = 0; i < HG; ++i) {
      float* rec = mrg + (wave * HG + i) * REC;
      *reinterpret_cast<f32x4*>(rec + 4 + 4 * c) = f32x4{acc[i][0], acc[i][1], acc[i][2], acc[i][3]};
    }
    if (c < HG) { float* rec = mrg + (wave * HG + c) * REC; *reinterpret_cast<f32x2*>(rec) = f32x2{m, l}; }
  }
  __syncthreads();
  if (wave >= HG) return;
  {
    const int hh = wave;
    f32x2 ml[WAVES], av[WAVES];
#pragma unroll
    for (int w = 0; w < WAVES; ++w) {
      const float* rec = mrg + (w * HG + hh) * REC;
      ml[w] = *reinterpret_cast<const f32x2*>(rec);
      av[w] = *reinterpret_cast<const f32x2*>(rec + 4 + 2 * lane);
    }
    float M = ml[0][0];
#pragma unroll
    for (int w = 1; w < WAVES; ++w) M = fmaxf(M, ml[w][0]);
    float L = 0.f, o0 = 0.f, o1 = 0.f;
#pragma unroll
    for (int w = 0; w < WAVES; ++w) {
      const float f = __builtin_amdgcn_exp2f(ml[w][0] - M);
      L = fmaf(f, ml[w][1], L);
      o0 = fmaf(f, av[w][0], o0);
      o1 = fmaf(f, av[w][1], o1);
    }
    const unsigned aoff = WS_ACC + ((((unsigned)(grp * HG + hh) * 4 + (lane >> 4)) * NSPLIT + split) * 256) + (lane & 15) * 16;
    __builtin_amdgcn_raw_buffer_store_b128(u32x4{__float_as_uint(o0), tag, __float_as_uint(o1), tag}, wrs, aoff, 0, 16);
    if (lane == 0) {
      const unsigned moff = WS_ML + ((unsigned)(grp * HG + hh) * NSPLIT + split) * 16;
      __builtin_amdgcn_raw_buffer_store_b128(u32x4{__float_as_uint(M), tag, __float_as_uint(L), tag}, wrs, moff, 0, 16);
    }
  }
  if (wave != 0) return;
  {
    const int hh = split >> 2, sl = split & 3;
    const unsigned abase = WS_ACC + (((unsigned)(grp * HG + hh) * 4 + sl) * NSPLIT) * 256 + lane * 16;
    const unsigned mbase = WS_ML + ((unsigned)(grp * HG + hh) * NSPLIT + (lane >> 4)) * 16;
    u32x4 av[4], mv[4];
    bool ok = false;
    for (unsigned spins = 0; spins < (1u << 18); ++spins) {
      asm volatile("" ::: "memory");
#pragma unroll
      for (int n = 0; n < 4; ++n) {
        av[n] = __builtin_amdgcn_raw_buffer_load_b128(wrs, abase + n * 1024, 0, 16);
        mv[n] = __builtin_amdgcn_raw_buffer_load_b128(wrs, mbase + n * 64, 0, 16);
      }
      unsigned bad = 0;
#pragma unroll
      for (int n = 0; n < 4; ++n) bad |= (av[n][1] ^ tag) | (av[n][3] ^ tag) | (mv[n][1] ^ tag) | (mv[n][3] ^ tag);
      if (__all(bad == 0)) { ok = true; break; }
      __builtin_amdgcn_s_sleep(2);
    }
    float M = -INFINITY;
#pragma unroll
    for (int n = 0; n < 4; ++n) M = fmaxf(M, __uint_as_float(mv[n][0]));
    M = x32_max(x16_max(M));
    float L = 0.f, o0 = 0.f, o1 = 0.f;
#pragma unroll
    for (int n = 0; n < 4; ++n) {
      const float f = __builtin_amdgcn_exp2f(__uint_as_float(mv[n][0]) - M);
      L = fmaf(f, __uint_as_float(mv[n][2]), L);
      o0 = fmaf(f, __uint_as_float(av[n][0]), o0);
      o1 = fmaf(f, __uint_as_float(av[n][2]), o1);
    }
    L = x32_sum(x16_sum(L));
    o0 = x32_sum(x16_sum(o0));
    o1 = x32_sum(x16_sum(o1));
    const float inv = ok ? 1.f / L : __uint_as_float(0x7fc00000u);
    if (lane < 16) {
      *reinterpret_cast<unsigned*>(a.out + (bh0 + hh) * D + sl * 32 + 2 * lane) = packbf(o0 * inv, o1 * inv);
    }
    if (split == 0 && lane == 0 && ok) __builtin_amdgcn_raw_buffer_store_b32(tag, wrs, WS_EPOCH + grp * 256, 0, 16);
  }
}

}

extern "C" void kernel_launch(void* const* d_in, const int* in_sizes, int n_in,
                              void* d_out, int out_size, void* d_ws, size_t ws_size,
                              hipStream_t stream) {
  Args a;
  a.q = (const unsigned short*)d_in[0];
  a.qk = (const int*)d_in[1];
  a.ksc = (const unsigned short*)d_in[2];
  a.kzp = (const unsigned short*)d_in[3];
  a.qv = (const int*)d_in[4];
  a.vsc = (const unsigned short*)d_in[5];
  a.vzp = (const unsigned short*)d_in[6];
  a.qcos = (const unsigned short*)d_in[7];
  a.qsin = (const unsigned short*)d_in[8];
  a.kcos = (const unsigned short*)d_in[9];
  a.ksin = (const unsigned short*)d_in[10];
  a.ws = (unsigned char*)d_ws;
  a.out = (__hip_bfloat16*)d_out;
  attn_decode<<<NWG, NTHR, 0, stream>>>(a);
}
```

```cpp
#include <hip/hip_runtime.h>
#include <hip/hip_bf16.h>
#include <stdint.h>

#define WAVES_ 4
#define NBUF 2
#define TABAUX 0
#define KAUX 2
#define VAUX 0
#define SCNT 0
#define LDSC(p) (*(p))
#define PROBAR 1
#define DYN 0

namespace {

constexpr int B_ = 2, H_ = 32, SKV = 4096, D = 128;
constexpr int BH = B_ * H_;
constexpr int HG = 4;
constexpr int NGRP = BH / HG;
constexpr int NWG = 256;
constexpr int NSPLIT = NWG / NGRP;
constexpr int WAVES = WAVES_;
constexpr int NTHR = WAVES * 64;
constexpr int TOK_WG = SKV / NSPLIT;
constexpr int NUNIT = TOK_WG / 2 / WAVES;
constexpr float SCALE_LOG2E = 0.088388347648318440f * 1.4426950408889634f;
constexpr unsigned HSTRIDE = (unsigned)SKV * D * 4;

constexpr unsigned WS_EPOCH = 0;
constexpr unsigned WS_ML = 4096;
constexpr unsigned WS_ACC = WS_ML + NGRP * HG * NSPLIT * 16;
constexpr unsigned WS_END = WS_ACC + NGRP * HG * 4 * NSPLIT * 256;

using u32x4 = __attribute__((ext_vector_type(4))) unsigned;
using u32x2 = __attribute__((ext_vector_type(2))) unsigned;
using f32x4 = __attribute__((ext_vector_type(4))) float;
using f32x2 = __attribute__((ext_vector_type(2))) float;
using bf16x2 = __attribute__((ext_vector_type(2))) __bf16;

__device__ __forceinline__ float rbf(float x) { return (float)(__bf16)x; }
__device__ __forceinline__ float bfu(unsigned short h) { return __uint_as_float((unsigned)h << 16); }
__device__ __forceinline__ unsigned packbf(float lo, float hi) {
  bf16x2 v; v[0] = (__bf16)lo; v[1] = (__bf16)hi;
  return __builtin_bit_cast(unsigned, v);
}
__device__ __forceinline__ float dot2(unsigned a, unsigned b, float acc) {
  return __builtin_amdgcn_fdot2_f32_bf16(__builtin_bit_cast(bf16x2, a), __builtin_bit_cast(bf16x2, b), acc, false);
}
template <int CTRL> __device__ __forceinline__ float dpp(float x) {
  return __builtin_bit_cast(float, __builtin_amdgcn_mov_dpp(__builtin_bit_cast(int, x), CTRL, 0xf, 0xf, true));
}
template <int I> __device__ __forceinline__ float qbc(float x) { return dpp<I * 0x55>(x); }
constexpr int XOR1 = 0xB1, XOR2 = 0x4E, ROR4 = 0x124, ROR8 = 0x128;
__device__ __forceinline__ float x16_sum(float x) {
  auto s = __builtin_amdgcn_permlane16_swap(__float_as_uint(x), __float_as_uint(x), false, false);
  return __uint_as_float(s[0]) + __uint_as_float(s[1]);
}
__device__ __forceinline__ float x16_max(float x) {
  auto s = __builtin_amdgcn_permlane16_swap(__float_as_uint(x), __float_as_uint(x), false, false);
  return fmaxf(__uint_as_float(s[0]), __uint_as_float(s[1]));
}
__device__ __forceinline__ float x32_sum(float x) {
  auto s = __builtin_amdgcn_permlane32_swap(__float_as_uint(x), __float_as_uint(x), false, false);
  return __uint_as_float(s[0]) + __uint_as_float(s[1]);
}
__device__ __forceinline__ float x32_max(float x) {
  auto s = __builtin_amdgcn_permlane32_swap(__float_as_uint(x), __float_as_uint(x), false, false);
  return fmaxf(__uint_as_float(s[0]), __uint_as_float(s[1]));
}

struct Args {
  const unsigned short* q;
  const int* qk;
  const unsigned short* ksc;
  const unsigned short* kzp;
  const int* qv;
  const unsigned short* vsc;
  const unsigned short* vzp;
  const unsigned short* qcos;
  const unsigned short* qsin;
  const unsigned short* kcos;
  const unsigned short* ksin;
  unsigned char* ws;
  __hip_bfloat16* out;
};

struct Unit {
  u32x4 k[HG];
  u32x4 v[HG];
  u32x2 c;
  u32x2 s;
};

constexpr int REC = 132;
constexpr int LDS_SC = 0;
constexpr int LDS_QAB = TOK_WG * HG * 4;
constexpr int LDS_MRG = LDS_QAB + HG * D;
constexpr int LDS_TICK = LDS_MRG + WAVES * HG * REC;
constexpr int LDS_FLOATS = 21 * 1024;
static_assert(LDS_TICK + 4 <= LDS_FLOATS, "");
constexpr int NUWG = TOK_WG / 2;
constexpr unsigned UOOB = 1u << 21;

__global__ void __launch_bounds__(NTHR) attn_decode(Args a) {
  __shared__ __attribute__((aligned(16))) float lds[LDS_FLOATS];
  f32x4* sc4 = reinterpret_cast<f32x4*>(lds + LDS_SC);
  unsigned* qabl = reinterpret_cast<unsigned*>(lds + LDS_QAB);
  float* mrg = lds + LDS_MRG;
  unsigned* tick = reinterpret_cast<unsigned*>(lds + LDS_TICK);
  const int split = blockIdx.x % NSPLIT;
  const int grp = blockIdx.x / NSPLIT;
  const int tid = threadIdx.x;
  const int lane = tid & 63;
  const int wave = __builtin_amdgcn_readfirstlane(tid >> 6);
  const int r = lane >> 5, c = lane & 31;
  const int cls = c & 3;
  const int bh0 = grp * HG;
  const int tw0 = split * TOK_WG;
  const int t0 = tw0;

  const auto krs = __builtin_amdgcn_make_buffer_rsrc((void*)(reinterpret_cast<const char*>(a.qk) + (size_t)bh0 * HSTRIDE), 0, HG * HSTRIDE, 0x00020000);
  const auto vrs = __builtin_amdgcn_make_buffer_rsrc((void*)(reinterpret_cast<const char*>(a.qv) + (size_t)bh0 * HSTRIDE), 0, HG * HSTRIDE, 0x00020000);
  const auto crs = __builtin_amdgcn_make_buffer_rsrc((void*)a.kcos, 0, SKV * D * 2, 0x00020000);
  const auto srs = __builtin_amdgcn_make_buffer_rsrc((void*)a.ksin, 0, SKV * D * 2, 0x00020000);
  const auto wrs = __builtin_amdgcn_make_buffer_rsrc((void*)a.ws, 0, WS_END, 0x00020000);
  const unsigned koff = (unsigned)t0 * (D * 4) + lane * 16;
  const unsigned coff = (unsigned)t0 * (D * 2) + lane * 8;
  const unsigned soff = (unsigned)t0 * (D * 2) + ((lane * 8) ^ 128);

  auto issue = [&](Unit& U, unsigned uu) {
    U.c = __builtin_amdgcn_raw_buffer_load_b64(crs, coff + uu * 512, 0, TABAUX);
    U.s = __builtin_amdgcn_raw_buffer_load_b64(srs, soff + uu * 512, 0, TABAUX);
#pragma unroll
    for (int i = 0; i < HG; ++i) U.k[i] = __builtin_amdgcn_raw_buffer_load_b128(krs, koff + uu * 1024, i * HSTRIDE, KAUX);
#pragma unroll
    for (int i = 0; i < HG; ++i) U.v[i] = __builtin_amdgcn_raw_buffer_load_b128(vrs, koff + uu * 1024, i * HSTRIDE, VAUX);
  };

  const int qh = wave & (HG - 1);
  const unsigned short* qp = a.q + (bh0 + qh) * D;
  const unsigned short q_lo = qp[lane], q_hi = qp[lane + 64];
  const unsigned short c_lo = a.qcos[lane], c_hi = a.qcos[lane + 64];
  const unsigned short s_lo = a.qsin[lane], s_hi = a.qsin[lane + 64];
  constexpr int NITEM = HG * TOK_WG / 2 / NTHR;
  unsigned sraw[NITEM][4];
#pragma unroll
  for (int j = 0; j < NITEM; ++j) {
    const int it = tid + NTHR * j;
    const int hh = it / (TOK_WG / 2), pp = it % (TOK_WG / 2);
    const size_t sidx = (size_t)(bh0 + hh) * SKV + tw0 + 2 * pp;
    sraw[j][0] = LDSC(reinterpret_cast<const unsigned*>(a.ksc + sidx));
    sraw[j][1] = LDSC(reinterpret_cast<const unsigned*>(a.kzp + sidx));
    sraw[j][2] = LDSC(reinterpret_cast<const unsigned*>(a.vsc + sidx));
    sraw[j][3] = LDSC(reinterpret_cast<const unsigned*>(a.vzp + sidx));
  }
  const unsigned tag = __builtin_amdgcn_raw_buffer_load_b32(wrs, WS_EPOCH + grp * 256, 0, 16) + 1u;
  if (tid == 0) *tick = NBUF * WAVES;
  __builtin_amdgcn_sched_barrier(0);
  __builtin_amdgcn_s_barrier();
  __builtin_amdgcn_sched_barrier(0);

  Unit U[NBUF];
  unsigned uid[NBUF];
#pragma unroll
  for (int b = 0; b < NBUF; ++b) { uid[b] = b * WAVES + wave; issue(U[b], uid[b]); __builtin_amdgcn_sched_barrier(0); }

  {
    const float q1 = bfu(q_lo), q2 = bfu(q_hi);
    const float r1 = rbf(rbf(q1 * bfu(c_lo)) + rbf(-q2 * bfu(s_lo)));
    const float r2 = rbf(rbf(q2 * bfu(c_hi)) + rbf(q1 * bfu(s_hi)));
    if (wave < HG) {
      qabl[qh * D + lane] = packbf(r1, r2);
      qabl[qh * D + lane + 64] = packbf(r2, -r1);
    }
  }
#pragma unroll
  for (int j = 0; j < NITEM; ++j) {
    const int it = tid + NTHR * j;
    const int hh = it / (TOK_WG / 2), pp = it % (TOK_WG / 2);
    f32x4 a0 = {__uint_as_float(sraw[j][0] << 16) * SCALE_LOG2E, __uint_as_float(sraw[j][1] << 16), __uint_as_float(sraw[j][2] << 16), __uint_as_float(sraw[j][3] << 16)};
    f32x4 a1 = {__uint_as_float(sraw[j][0] & 0xffff0000u) * SCALE_LOG2E, __uint_as_float(sraw[j][1] & 0xffff0000u), __uint_as_float(sraw[j][2] & 0xffff0000u), __uint_as_float(sraw[j][3] & 0xffff0000u)};
    sc4[(2 * pp) * HG + hh] = a0;
    sc4[(2 * pp + 1) * HG + hh] = a1;
  }
  __syncthreads();
  u32x4 qab[HG];
#pragma unroll
  for (int i = 0; i < HG; ++i) qab[i] = *reinterpret_cast<const u32x4*>(qabl + i * D + 4 * c);

  float m = -INFINITY, l = 0.f, corr = 0.f;
  float acc[HG][4];
#pragma unroll
  for (int i = 0; i < HG; ++i)
#pragma unroll
    for (int j = 0; j < 4; ++j) acc[i][j] = 0.f;
  const bool b0 = c & 1, b1 = c & 2;

  auto compute = [&](const Unit& U, unsigned u) {
    const f32x4 sc = sc4[(2 * u + r) * HG + cls];
    unsigned cs[4];
    cs[0] = __builtin_amdgcn_perm(U.s[0], U.c[0], 0x05040100u);
    cs[1] = __builtin_amdgcn_perm(U.s[0], U.c[0], 0x07060302u);
    cs[2] = __builtin_amdgcn_perm(U.s[1], U.c[1], 0x05040100u);
    cs[3] = __builtin_amdgcn_perm(U.s[1], U.c[1], 0x07060302u);
    float x[HG];
#pragma unroll
    for (int i = 0; i < HG; ++i) {
      const float kz = i == 0 ? qbc<0>(sc[1]) : i == 1 ? qbc<1>(sc[1]) : i == 2 ? qbc<2>(sc[1]) : qbc<3>(sc[1]);
      float d = 0.f;
#pragma unroll
      for (int j = 0; j < 4; ++j) {
        const float w = dot2(cs[j], qab[i][j], 0.f);
        d = fmaf((float)(int)U.k[i][j] - kz, w, d);
      }
      x[i] = d;
    }
    const float e0 = x[0] + dpp<XOR1>(x[0]), e1 = x[1] + dpp<XOR1>(x[1]);
    const float e2 = x[2] + dpp<XOR1>(x[2]), e3 = x[3] + dpp<XOR1>(x[3]);
    const float y0 = b0 ? e1 : e0;
    const float y1 = b0 ? e3 : e2;
    const float g0 = y0 + dpp<XOR2>(y0), g1 = y1 + dpp<XOR2>(y1);
    float z = b1 ? g1 : g0;
    z += dpp<ROR4>(z);
    z += dpp<ROR8>(z);
    z = x16_sum(z);
    const float s = z * sc[0];
    const float mn = fmaxf(m, s);
    const float alpha = __builtin_amdgcn_exp2f(m - mn);
    const float p = __builtin_amdgcn_exp2f(s - mn);
    m = mn;
    l = fmaf(l, alpha, p);
    const float pv = p * sc[2];
    corr = fmaf(corr, alpha, pv * sc[3]);
#pragma unroll
    for (int i = 0; i < HG; ++i) {
      const float al = i == 0 ? qbc<0>(alpha) : i == 1 ? qbc<1>(alpha) : i == 2 ? qbc<2>(alpha) : qbc<3>(alpha);
      const float pi = i == 0 ? qbc<0>(pv) : i == 1 ? qbc<1>(pv) : i == 2 ? qbc<2>(pv) : qbc<3>(pv);
#pragma unroll
      for (int j = 0; j < 4; ++j) acc[i][j] = fmaf(pi, (float)(int)U.v[i][j], acc[i][j] * al);
    }
  };

  constexpr int NMAIN = ((NUNIT - NBUF) / NBUF) * NBUF;
#pragma unroll 1
  for (int u = 0; u < NMAIN; u += NBUF) {
#pragma unroll
    for (int b = 0; b < NBUF; ++b) {
      compute(U[b], (u + b) * WAVES + wave);
      __builtin_amdgcn_sched_barrier(0);
      issue(U[b], (u + b + NBUF) * WAVES + wave);
      __builtin_amdgcn_sched_barrier(0);
    }
  }
#pragma unroll
  for (int u = NMAIN; u < NUNIT; ++u) {
    compute(U[u % NBUF], u * WAVES + wave);
    __builtin_amdgcn_sched_barrier(0);
    if (u + NBUF < NUNIT) { issue(U[u % NBUF], (u + NBUF) * WAVES + wave); __builtin_amdgcn_sched_barrier(0); }
  }

  {
    const float M = x32_max(m);
    const float f = __builtin_amdgcn_exp2f(m - M);
    l = x32_sum(l * f);
#pragma unroll
    for (int i = 0; i < HG; ++i) {
      const float fi = i == 0 ? qbc<0>(f) : i == 1 ? qbc<1>(f) : i == 2 ? qbc<2>(f) : qbc<3>(f);
      const float ci = i == 0 ? qbc<0>(corr) : i == 1 ? qbc<1>(corr) : i == 2 ? qbc<2>(corr) : qbc<3>(corr);
#pragma unroll
      for (int j = 0; j < 4; ++j) acc[i][j] = x32_sum((acc[i][j] - ci) * fi);
    }
    m = M;
  }
  if (r == 0) {
#pragma unroll
    for (int i = 0; i < HG; ++i) {
      float* rec = mrg + (wave * HG + i) * REC;
      *reinterpret_cast<f32x4*>(rec + 4 + 4 * c) = f32x4{acc[i][0], acc[i][1], acc[i][2], acc[i][3]};
    }
    if (c < HG) { float* rec = mrg + (wave * HG + c) * REC; *reinterpret_cast<f32x2*>(rec) = f32x2{m, l}; }
  }
  __syncthreads();
  if (wave >= HG) return;
  {
    const int hh = wave;
    f32x2 ml[WAVES], av[WAVES];
#pragma unroll
    for (int w = 0; w < WAVES; ++w) {
      const float* rec = mrg + (w * HG + hh) * REC;
      ml[w] = *reinterpret_cast<const f32x2*>(rec);
      av[w] = *reinterpret_cast<const f32x2*>(rec + 4 + 2 * lane);
    }
    float M = ml[0][0];
#pragma unroll
    for (int w = 1; w < WAVES; ++w) M = fmaxf(M, ml[w][0]);
    float L = 0.f, o0 = 0.f, o1 = 0.f;
#pragma unroll
    for (int w = 0; w < WAVES; ++w) {
      const float f = __builtin_amdgcn_exp2f(ml[w][0] - M);
      L = fmaf(f, ml[w][1], L);
      o0 = fmaf(f, av[w][0], o0);
      o1 = fmaf(f, av[w][1], o1);
    }
    const unsigned aoff = WS_ACC + ((((unsigned)(grp * HG + hh) * 4 + (lane >> 4)) * NSPLIT + split) * 256) + (lane & 15) * 16;
    __builtin_amdgcn_raw_buffer_store_b128(u32x4{__float_as_uint(o0), tag, __float_as_uint(o1), tag}, wrs, aoff, 0, 16);
    if (lane == 0) {
      const unsigned moff = WS_ML + ((unsigned)(grp * HG + hh) * NSPLIT + split) * 16;
      __builtin_amdgcn_raw_buffer_store_b128(u32x4{__float_as_uint(M), tag, __float_as_uint(L), tag}, wrs, moff, 0, 16);
    }
  }
  if (wave != 0) return;
  {
    const int hh = split >> 2, sl = split & 3;
    const unsigned abase = WS_ACC + (((unsigned)(grp * HG + hh) * 4 + sl) * NSPLIT) * 256 + lane * 16;
    const unsigned mbase = WS_ML + ((unsigned)(grp * HG + hh) * NSPLIT + (lane >> 4)) * 16;
    u32x4 av[4], mv[4];
    bool ok = false;
    for (unsigned spins = 0; spins < (1u << 18); ++spins) {
      asm volatile("" ::: "memory");
#pragma unroll
      for (int n = 0; n < 4; ++n) {
        av[n] = __builtin_amdgcn_raw_buffer_load_b128(wrs, abase + n * 1024, 0, 16);
        mv[n] = __builtin_amdgcn_raw_buffer_load_b128(wrs, mbase + n * 64, 0, 16);
      }
      unsigned bad = 0;
#pragma unroll
      for (int n = 0; n < 4; ++n) bad |= (av[n][1] ^ tag) | (av[n][3] ^ tag) | (mv[n][1] ^ tag) | (mv[n][3] ^ tag);
      if (__all(bad == 0)) { ok = true; break; }
      __builtin_amdgcn_s_sleep(2);
    }
    float M = -INFINITY;
#pragma unroll
    for (int n = 0; n < 4; ++n) M = fmaxf(M, __uint_as_float(mv[n][0]));
    M = x32_max(x16_max(M));
    float L = 0.f, o0 = 0.f, o1 = 0.f;
#pragma unroll
    for (int n = 0; n < 4; ++n) {
      const float f = __builtin_amdgcn_exp2f(__uint_as_float(mv[n][0]) - M);
      L = fmaf(f, __uint_as_float(mv[n][2]), L);
      o0 = fmaf(f, __uint_as_float(av[n][0]), o0);
      o1 = fmaf(f, __uint_as_float(av[n][2]), o1);
    }
    L = x32_sum(x16_sum(L));
    o0 = x32_sum(x16_sum(o0));
    o1 = x32_sum(x16_sum(o1));
    const float inv = ok ? 1.f / L : __uint_as_float(0x7fc00000u);
    if (lane < 16) {
      *reinterpret_cast<unsigned*>(a.out + (bh0 + hh) * D + sl * 32 + 2 * lane) = packbf(o0 * inv, o1 * inv);
    }
    if (split == 0 && lane == 0 && ok) __builtin_amdgcn_raw_buffer_store_b32(tag, wrs, WS_EPOCH + grp * 256, 0, 16);
  }
}

}

extern "C" void kernel_launch(void* const* d_in, const int* in_sizes, int n_in,
                              void* d_out, int out_size, void* d_ws, size_t ws_size,
                              hipStream_t stream) {
  Args a;
  a.q = (const unsigned short*)d_in[0];
  a.qk = (const int*)d_in[1];
  a.ksc = (const unsigned short*)d_in[2];
  a.kzp = (const unsigned short*)d_in[3];
  a.qv = (const int*)d_in[4];
  a.vsc = (const unsigned short*)d_in[5];
  a.vzp = (const unsigned short*)d_in[6];
  a.qcos = (const unsigned short*)d_in[7];
  a.qsin = (const unsigned short*)d_in[8];
  a.kcos = (const unsigned short*)d_in[9];
  a.ksin = (const unsigned short*)d_in[10];
  a.ws = (unsigned char*)d_ws;
  a.out = (__hip_bfloat16*)d_out;
  attn_decode<<<NWG, NTHR, 0, stream>>>(a);
}
```

```cpp
#include <hip/hip_runtime.h>
#include <hip/hip_bf16.h>
#include <stdint.h>
#include <stdio.h>
#include <vector>
#include <algorithm>
#define STAMP 1

#define WAVES_ 4
#define NBUF 2
#define TABAUX 2
#define KAUX 2
#define VAUX 2
#define SCNT 1
#define LDSC(p) __builtin_nontemporal_load(p)
#define PROBAR 1
#define DYN 0

namespace {

constexpr int B_ = 2, H_ = 32, SKV = 4096, D = 128;
constexpr int BH = B_ * H_;
constexpr int HG = 4;
constexpr int NGRP = BH / HG;
constexpr int NWG = 256;
constexpr int NSPLIT = NWG / NGRP;
constexpr int WAVES = WAVES_;
constexpr int NTHR = WAVES * 64;
constexpr int TOK_WG = SKV / NSPLIT;
constexpr int NUNIT = TOK_WG / 2 / WAVES;
constexpr float SCALE_LOG2E = 0.088388347648318440f * 1.4426950408889634f;
constexpr unsigned HSTRIDE = (unsigned)SKV * D * 4;

constexpr unsigned WS_EPOCH = 0;
constexpr unsigned WS_ML = 4096;
constexpr unsigned WS_ACC = WS_ML + NGRP * HG * NSPLIT * 16;
constexpr unsigned WS_END = WS_ACC + NGRP * HG * 4 * NSPLIT * 256;

using u32x4 = __attribute__((ext_vector_type(4))) unsigned;
using u32x2 = __attribute__((ext_vector_type(2))) unsigned;
using f32x4 = __attribute__((ext_vector_type(4))) float;
using f32x2 = __attribute__((ext_vector_type(2))) float;
using bf16x2 = __attribute__((ext_vector_type(2))) __bf16;

__device__ __forceinline__ float rbf(float x) { return (float)(__bf16)x; }
__device__ __forceinline__ float bfu(unsigned short h) { return __uint_as_float((unsigned)h << 16); }
__device__ __forceinline__ unsigned packbf(float lo, float hi) {
  bf16x2 v; v[0] = (__bf16)lo; v[1] = (__bf16)hi;
  return __builtin_bit_cast(unsigned, v);
}
__device__ __forceinline__ float dot2(unsigned a, unsigned b, float acc) {
  return __builtin_amdgcn_fdot2_f32_bf16(__builtin_bit_cast(bf16x2, a), __builtin_bit_cast(bf16x2, b), acc, false);
}
template <int CTRL> __device__ __forceinline__ float dpp(float x) {
  return __builtin_bit_cast(float, __builtin_amdgcn_mov_dpp(__builtin_bit_cast(int, x), CTRL, 0xf, 0xf, true));
}
template <int I> __device__ __forceinline__ float qbc(float x) { return dpp<I * 0x55>(x); }
constexpr int XOR1 = 0xB1, XOR2 = 0x4E, ROR4 = 0x124, ROR8 = 0x128;
__device__ __forceinline__ float x16_sum(float x) {
  auto s = __builtin_amdgcn_permlane16_swap(__float_as_uint(x), __float_as_uint(x), false, false);
  return __uint_as_float(s[0]) + __uint_as_float(s[1]);
}
__device__ __forceinline__ float x16_max(float x) {
  auto s = __builtin_amdgcn_permlane16_swap(__float_as_uint(x), __float_as_uint(x), false, false);
  return fmaxf(__uint_as_float(s[0]), __uint_as_float(s[1]));
}
__device__ __forceinline__ float x32_sum(float x) {
  auto s = __builtin_amdgcn_permlane32_swap(__float_as_uint(x), __float_as_uint(x), false, false);
  return __uint_as_float(s[0]) + __uint_as_float(s[1]);
}
__device__ __forceinline__ float x32_max(float x) {
  auto s = __builtin_amdgcn_permlane32_swap(__float_as_uint(x), __float_as_uint(x), false, false);
  return fmaxf(__uint_as_float(s[0]), __uint_as_float(s[1]));
}

struct Args {
  const unsigned short* q;
  const int* qk;
  const unsigned short* ksc;
  const unsigned short* kzp;
  const int* qv;
  const unsigned short* vsc;
  const unsigned short* vzp;
  const unsigned short* qcos;
  const unsigned short* qsin;
  const unsigned short* kcos;
  const unsigned short* ksin;
  unsigned char* ws;
  __hip_bfloat16* out;
};

struct Unit {
  u32x4 k[HG];
  u32x4 v[HG];
  u32x2 c;
  u32x2 s;
};

constexpr int REC = 132;
constexpr int LDS_SC = 0;
constexpr int LDS_QAB = TOK_WG * HG * 4;
constexpr int LDS_MRG = LDS_QAB + HG * D;
constexpr int LDS_TICK = LDS_MRG + WAVES * HG * REC;
constexpr int LDS_FLOATS = 21 * 1024;
static_assert(LDS_TICK + 4 <= LDS_FLOATS, "");
constexpr int NUWG = TOK_WG / 2;
constexpr unsigned UOOB = 1u << 21;

__global__ void __launch_bounds__(NTHR) attn_decode(Args a) {
  __shared__ __attribute__((aligned(16))) float lds[LDS_FLOATS];
  f32x4* sc4 = reinterpret_cast<f32x4*>(lds + LDS_SC);
  unsigned* qabl = reinterpret_cast<unsigned*>(lds + LDS_QAB);
  float* mrg = lds + LDS_MRG;
  unsigned* tick = reinterpret_cast<unsigned*>(lds + LDS_TICK);
  const unsigned long long t_start = __builtin_amdgcn_s_memrealtime();
  const int split = blockIdx.x % NSPLIT;
  const int grp = blockIdx.x / NSPLIT;
  const int tid = threadIdx.x;
  const int lane = tid & 63;
  const int wave = __builtin_amdgcn_readfirstlane(tid >> 6);
  const int r = lane >> 5, c = lane & 31;
  const int cls = c & 3;
  const int bh0 = grp * HG;
  const int tw0 = split * TOK_WG;
  const int t0 = tw0;

  const auto krs = __builtin_amdgcn_make_buffer_rsrc((void*)(reinterpret_cast<const char*>(a.qk) + (size_t)bh0 * HSTRIDE), 0, HG * HSTRIDE, 0x00020000);
  const auto vrs = __builtin_amdgcn_make_buffer_rsrc((void*)(reinterpret_cast<const char*>(a.qv) + (size_t)bh0 * HSTRIDE), 0, HG * HSTRIDE, 0x00020000);
  const auto crs = __builtin_amdgcn_make_buffer_rsrc((void*)a.kcos, 0, SKV * D * 2, 0x00020000);
  const auto srs = __builtin_amdgcn_make_buffer_rsrc((void*)a.ksin, 0, SKV * D * 2, 0x00020000);
  const auto wrs = __builtin_amdgcn_make_buffer_rsrc((void*)a.ws, 0, WS_END, 0x00020000);
  const unsigned koff = (unsigned)t0 * (D * 4) + lane * 16;
  const unsigned coff = (unsigned)t0 * (D * 2) + lane * 8;
  const unsigned soff = (unsigned)t0 * (D * 2) + ((lane * 8) ^ 128);

  auto issue = [&](Unit& U, unsigned uu) {
    U.c = __builtin_amdgcn_raw_buffer_load_b64(crs, coff + uu * 512, 0, TABAUX);
    U.s = __builtin_amdgcn_raw_buffer_load_b64(srs, soff + uu * 512, 0, TABAUX);
#pragma unroll
    for (int i = 0; i < HG; ++i) U.k[i] = __builtin_amdgcn_raw_buffer_load_b128(krs, koff + uu * 1024, i * HSTRIDE, KAUX);
#pragma unroll
    for (int i = 0; i < HG; ++i) U.v[i] = __builtin_amdgcn_raw_buffer_load_b128(vrs, koff + uu * 1024, i * HSTRIDE, VAUX);
  };

  const int qh = wave & (HG - 1);
  const unsigned short* qp = a.q + (bh0 + qh) * D;
  const unsigned short q_lo = qp[lane], q_hi = qp[lane + 64];
  const unsigned short c_lo = a.qcos[lane], c_hi = a.qcos[lane + 64];
  const unsigned short s_lo = a.qsin[lane], s_hi = a.qsin[lane + 64];
  constexpr int NITEM = HG * TOK_WG / 2 / NTHR;
  unsigned sraw[NITEM][4];
#pragma unroll
  for (int j = 0; j < NITEM; ++j) {
    const int it = tid + NTHR * j;
    const int hh = it / (TOK_WG / 2), pp = it % (TOK_WG / 2);
    const size_t sidx = (size_t)(bh0 + hh) * SKV + tw0 + 2 * pp;
    sraw[j][0] = LDSC(reinterpret_cast<const unsigned*>(a.ksc + sidx));
    sraw[j][1] = LDSC(reinterpret_cast<const unsigned*>(a.kzp + sidx));
    sraw[j][2] = LDSC(reinterpret_cast<const unsigned*>(a.vsc + sidx));
    sraw[j][3] = LDSC(reinterpret_cast<const unsigned*>(a.vzp + sidx));
  }
  const unsigned tag = __builtin_amdgcn_raw_buffer_load_b32(wrs, WS_EPOCH + grp * 256, 0, 16) + 1u;
  if (tid == 0) *tick = NBUF * WAVES;
  __builtin_amdgcn_sched_barrier(0);
  __builtin_amdgcn_s_barrier();
  __builtin_amdgcn_sched_barrier(0);

  Unit U[NBUF];
  unsigned uid[NBUF];
#pragma unroll
  for (int b = 0; b < NBUF; ++b) { uid[b] = b * WAVES + wave; issue(U[b], uid[b]); __builtin_amdgcn_sched_barrier(0); }

  {
    const float q1 = bfu(q_lo), q2 = bfu(q_hi);
    const float r1 = rbf(rbf(q1 * bfu(c_lo)) + rbf(-q2 * bfu(s_lo)));
    const float r2 = rbf(rbf(q2 * bfu(c_hi)) + rbf(q1 * bfu(s_hi)));
    if (wave < HG) {
      qabl[qh * D + lane] = packbf(r1, r2);
      qabl[qh * D + lane + 64] = packbf(r2, -r1);
    }
  }
#pragma unroll
  for (int j = 0; j < NITEM; ++j) {
    const int it = tid + NTHR * j;
    const int hh = it / (TOK_WG / 2), pp = it % (TOK_WG / 2);
    f32x4 a0 = {__uint_as_float(sraw[j][0] << 16) * SCALE_LOG2E, __uint_as_float(sraw[j][1] << 16), __uint_as_float(sraw[j][2] << 16), __uint_as_float(sraw[j][3] << 16)};
    f32x4 a1 = {__uint_as_float(sraw[j][0] & 0xffff0000u) * SCALE_LOG2E, __uint_as_float(sraw[j][1] & 0xffff0000u), __uint_as_float(sraw[j][2] & 0xffff0000u), __uint_as_float(sraw[j][3] & 0xffff0000u)};
    sc4[(2 * pp) * HG + hh] = a0;
    sc4[(2 * pp + 1) * HG + hh] = a1;
  }
  __syncthreads();
  u32x4 qab[HG];
#pragma unroll
  for (int i = 0; i < HG; ++i) qab[i] = *reinterpret_cast<const u32x4*>(qabl + i * D + 4 * c);
  const unsigned long long t_pro = __builtin_amdgcn_s_memrealtime();

  float m = -INFINITY, l = 0.f, corr = 0.f;
  float acc[HG][4];
#pragma unroll
  for (int i = 0; i < HG; ++i)
#pragma unroll
    for (int j = 0; j < 4; ++j) acc[i][j] = 0.f;
  const bool b0 = c & 1, b1 = c & 2;

  auto compute = [&](const Unit& U, unsigned u) {
    const f32x4 sc = sc4[(2 * u + r) * HG + cls];
    unsigned cs[4];
    cs[0] = __builtin_amdgcn_perm(U.s[0], U.c[0], 0x05040100u);
    cs[1] = __builtin_amdgcn_perm(U.s[0], U.c[0], 0x07060302u);
    cs[2] = __builtin_amdgcn_perm(U.s[1], U.c[1], 0x05040100u);
    cs[3] = __builtin_amdgcn_perm(U.s[1], U.c[1], 0x07060302u);
    float x[HG];
#pragma unroll
    for (int i = 0; i < HG; ++i) {
      const float kz = i == 0 ? qbc<0>(sc[1]) : i == 1 ? qbc<1>(sc[1]) : i == 2 ? qbc<2>(sc[1]) : qbc<3>(sc[1]);
      float d = 0.f;
#pragma unroll
      for (int j = 0; j < 4; ++j) {
        const float w = dot2(cs[j], qab[i][j], 0.f);
        d = fmaf((float)(int)U.k[i][j] - kz, w, d);
      }
      x[i] = d;
    }
    const float e0 = x[0] + dpp<XOR1>(x[0]), e1 = x[1] + dpp<XOR1>(x[1]);
    const float e2 = x[2] + dpp<XOR1>(x[2]), e3 = x[3] + dpp<XOR1>(x[3]);
    const float y0 = b0 ? e1 : e0;
    const float y1 = b0 ? e3 : e2;
    const float g0 = y0 + dpp<XOR2>(y0), g1 = y1 + dpp<XOR2>(y1);
    float z = b1 ? g1 : g0;
    z += dpp<ROR4>(z);
    z += dpp<ROR8>(z);
    z = x16_sum(z);
    const float s = z * sc[0];
    const float mn = fmaxf(m, s);
    const float alpha = __builtin_amdgcn_exp2f(m - mn);
    const float p = __builtin_amdgcn_exp2f(s - mn);
    m = mn;
    l = fmaf(l, alpha, p);
    const float pv = p * sc[2];
    corr = fmaf(corr, alpha, pv * sc[3]);
#pragma unroll
    for (int i = 0; i < HG; ++i) {
      const float al = i == 0 ? qbc<0>(alpha) : i == 1 ? qbc<1>(alpha) : i == 2 ? qbc<2>(alpha) : qbc<3>(alpha);
      const float pi = i == 0 ? qbc<0>(pv) : i == 1 ? qbc<1>(pv) : i == 2 ? qbc<2>(pv) : qbc<3>(pv);
#pragma unroll
      for (int j = 0; j < 4; ++j) acc[i][j] = fmaf(pi, (float)(int)U.v[i][j], acc[i][j] * al);
    }
  };

  constexpr int NMAIN = ((NUNIT - NBUF) / NBUF) * NBUF;
#pragma unroll 1
  for (int u = 0; u < NMAIN; u += NBUF) {
#pragma unroll
    for (int b = 0; b < NBUF; ++b) {
      compute(U[b], (u + b) * WAVES + wave);
      __builtin_amdgcn_sched_barrier(0);
      issue(U[b], (u + b + NBUF) * WAVES + wave);
      __builtin_amdgcn_sched_barrier(0);
    }
  }
#pragma unroll
  for (int u = NMAIN; u < NUNIT; ++u) {
    compute(U[u % NBUF], u * WAVES + wave);
    __builtin_amdgcn_sched_barrier(0);
    if (u + NBUF < NUNIT) { issue(U[u % NBUF], (u + NBUF) * WAVES + wave); __builtin_amdgcn_sched_barrier(0); }
  }
  asm volatile("" : "+v"(m), "+v"(l), "+v"(acc[0][0]), "+v"(acc[3][3]));
  const unsigned long long t_loop = __builtin_amdgcn_s_memrealtime();

  {
    const float M = x32_max(m);
    const float f = __builtin_amdgcn_exp2f(m - M);
    l = x32_sum(l * f);
#pragma unroll
    for (int i = 0; i < HG; ++i) {
      const float fi = i == 0 ? qbc<0>(f) : i == 1 ? qbc<1>(f) : i == 2 ? qbc<2>(f) : qbc<3>(f);
      const float ci = i == 0 ? qbc<0>(corr) : i == 1 ? qbc<1>(corr) : i == 2 ? qbc<2>(corr) : qbc<3>(corr);
#pragma unroll
      for (int j = 0; j < 4; ++j) acc[i][j] = x32_sum((acc[i][j] - ci) * fi);
    }
    m = M;
  }
  if (r == 0) {
#pragma unroll
    for (int i = 0; i < HG; ++i) {
      float* rec = mrg + (wave * HG + i) * REC;
      *reinterpret_cast<f32x4*>(rec + 4 + 4 * c) = f32x4{acc[i][0], acc[i][1], acc[i][2], acc[i][3]};
    }
    if (c < HG) { float* rec = mrg + (wave * HG + c) * REC; *reinterpret_cast<f32x2*>(rec) = f32x2{m, l}; }
  }
  __syncthreads();
  if (wave >= HG) return;
  {
    const int hh = wave;
    f32x2 ml[WAVES], av[WAVES];
#pragma unroll
    for (int w = 0; w < WAVES; ++w) {
      const float* rec = mrg + (w * HG + hh) * REC;
      ml[w] = *reinterpret_cast<const f32x2*>(rec);
      av[w] = *reinterpret_cast<const f32x2*>(rec + 4 + 2 * lane);
    }
    float M = ml[0][0];
#pragma unroll
    for (int w = 1; w < WAVES; ++w) M = fmaxf(M, ml[w][0]);
    float L = 0.f, o0 = 0.f, o1 = 0.f;
#pragma unroll
    for (int w = 0; w < WAVES; ++w) {
      const float f = __builtin_amdgcn_exp2f(ml[w][0] - M);
      L = fmaf(f, ml[w][1], L);
      o0 = fmaf(f, av[w][0], o0);
      o1 = fmaf(f, av[w][1], o1);
    }
    const unsigned aoff = WS_ACC + ((((unsigned)(grp * HG + hh) * 4 + (lane >> 4)) * NSPLIT + split) * 256) + (lane & 15) * 16;
    __builtin_amdgcn_raw_buffer_store_b128(u32x4{__float_as_uint(o0), tag, __float_as_uint(o1), tag}, wrs, aoff, 0, 16);
    if (lane == 0) {
      const unsigned moff = WS_ML + ((unsigned)(grp * HG + hh) * NSPLIT + split) * 16;
      __builtin_amdgcn_raw_buffer_store_b128(u32x4{__float_as_uint(M), tag, __float_as_uint(L), tag}, wrs, moff, 0, 16);
    }
  }
  if (wave != 0) return;
  const unsigned long long t_pub = __builtin_amdgcn_s_memrealtime();
  {
    const int hh = split >> 2, sl = split & 3;
    const unsigned abase = WS_ACC + (((unsigned)(grp * HG + hh) * 4 + sl) * NSPLIT) * 256 + lane * 16;
    const unsigned mbase = WS_ML + ((unsigned)(grp * HG + hh) * NSPLIT + (lane >> 4)) * 16;
    u32x4 av[4], mv[4];
    bool ok = false;
    for (unsigned spins = 0; spins < (1u << 18); ++spins) {
      asm volatile("" ::: "memory");
#pragma unroll
      for (int n = 0; n < 4; ++n) {
        av[n] = __builtin_amdgcn_raw_buffer_load_b128(wrs, abase + n * 1024, 0, 16);
        mv[n] = __builtin_amdgcn_raw_buffer_load_b128(wrs, mbase + n * 64, 0, 16);
      }
      unsigned bad = 0;
#pragma unroll
      for (int n = 0; n < 4; ++n) bad |= (av[n][1] ^ tag) | (av[n][3] ^ tag) | (mv[n][1] ^ tag) | (mv[n][3] ^ tag);
      if (__all(bad == 0)) { ok = true; break; }
      __builtin_amdgcn_s_sleep(2);
    }
    float M = -INFINITY;
#pragma unroll
    for (int n = 0; n < 4; ++n) M = fmaxf(M, __uint_as_float(mv[n][0]));
    M = x32_max(x16_max(M));
    float L = 0.f, o0 = 0.f, o1 = 0.f;
#pragma unroll
    for (int n = 0; n < 4; ++n) {
      const float f = __builtin_amdgcn_exp2f(__uint_as_float(mv[n][0]) - M);
      L = fmaf(f, __uint_as_float(mv[n][2]), L);
      o0 = fmaf(f, __uint_as_float(av[n][0]), o0);
      o1 = fmaf(f, __uint_as_float(av[n][2]), o1);
    }
    L = x32_sum(x16_sum(L));
    o0 = x32_sum(x16_sum(o0));
    o1 = x32_sum(x16_sum(o1));
    const float inv = ok ? 1.f / L : __uint_as_float(0x7fc00000u);
    if (lane < 16) {
      *reinterpret_cast<unsigned*>(a.out + (bh0 + hh) * D + sl * 32 + 2 * lane) = packbf(o0 * inv, o1 * inv);
    }
    if (split == 0 && lane == 0 && ok) __builtin_amdgcn_raw_buffer_store_b32(tag, wrs, WS_EPOCH + grp * 256, 0, 16);
  }
  const unsigned long long t_end = __builtin_amdgcn_s_memrealtime();
  if (lane == 0) { unsigned long long* sp = reinterpret_cast<unsigned long long*>(a.ws + (256u << 20)) + 8 * ((size_t)blockIdx.x * WAVES + wave); sp[0] = t_start; sp[1] = t_pro; sp[2] = t_loop; sp[3] = t_pub; sp[4] = t_end; }
}

}

extern "C" void kernel_launch(void* const* d_in, const int* in_sizes, int n_in,
                              void* d_out, int out_size, void* d_ws, size_t ws_size,
                              hipStream_t stream) {
  Args a;
  a.q = (const unsigned short*)d_in[0];
  a.qk = (const int*)d_in[1];
  a.ksc = (const unsigned short*)d_in[2];
  a.kzp = (const unsigned short*)d_in[3];
  a.qv = (const int*)d_in[4];
  a.vsc = (const unsigned short*)d_in[5];
  a.vzp = (const unsigned short*)d_in[6];
  a.qcos = (const unsigned short*)d_in[7];
  a.qsin = (const unsigned short*)d_in[8];
  a.kcos = (const unsigned short*)d_in[9];
  a.ksin = (const unsigned short*)d_in[10];
  a.ws = (unsigned char*)d_ws;
  a.out = (__hip_bfloat16*)d_out;
  attn_decode<<<NWG, NTHR, 0, stream>>>(a);
  hipStreamCaptureStatus cs; (void)hipStreamIsCapturing(stream, &cs);
  if (cs == hipStreamCaptureStatusNone) {
    auto analyze = [&](const char* tag) {
      (void)hipStreamSynchronize(stream);
      const int nw = NWG * WAVES, NS = 8;
      std::vector<unsigned long long> stp(NS * nw);
      (void)hipMemcpy(stp.data(), (char*)d_ws + (256u << 20), nw * NS * 8, hipMemcpyDeviceToHost);
      unsigned long long mn = ~0ull, mx = 0;
      for (int i = 0; i < nw; i += WAVES) { mn = std::min(mn, stp[NS * i]); mx = std::max(mx, stp[NS * i + 4]); }
      fprintf(stderr, "DIAG %s span %.2f us\n", tag, (mx - mn) * 0.01);
    };
    analyze("first");
    for (int i = 0; i < 11; ++i) { hipPointerAttribute_t at; if (hipPointerGetAttributes(&at, d_in[i]) == hipSuccess) fprintf(stderr, "DIAG in%d ptr %p type %d managed %d flags %u\n", i, d_in[i], (int)at.type, at.isManaged, at.allocationFlags); }
    fprintf(stderr, "DIAG ws %p out %p\n", d_ws, d_out);
    for (int rep = 0; rep < 4; ++rep) {
      hipEvent_t e0, e1; (void)hipEventCreate(&e0); (void)hipEventCreate(&e1);
      for (int i = 0; i < 5; ++i) attn_decode<<<NWG, NTHR, 0, stream>>>(a);
      (void)hipEventRecord(e0, stream);
      for (int i = 0; i < 50; ++i) attn_decode<<<NWG, NTHR, 0, stream>>>(a);
      (void)hipEventRecord(e1, stream);
      (void)hipStreamSynchronize(stream);
      float ms; (void)hipEventElapsedTime(&ms, e0, e1);
      fprintf(stderr, "DIAG back-to-back plain launches: %.2f us each\n", ms * 1000 / 50);
      analyze("warm");
    }
  }
}
```

```cpp
#include <hip/hip_runtime.h>
#include <hip/hip_bf16.h>
#include <stdint.h>

#define WAVES_ 4
#define TABAUX 2
#define KAUX 2
#define VAUX 2
#define SCNT 1
#define LDSC(p) __builtin_nontemporal_load(p)
#define ROLL 1
#define PSLEEP 1

namespace {

constexpr int B_ = 2, H_ = 32, SKV = 4096, D = 128;
constexpr int BH = B_ * H_;
constexpr int HG = 4;
constexpr int NGRP = BH / HG;
constexpr int NWG = 256;
constexpr int NSPLIT = NWG / NGRP;
constexpr int WAVES = WAVES_;
constexpr int NTHR = WAVES * 64;
constexpr int TOK_WG = SKV / NSPLIT;
constexpr int NUNIT = TOK_WG / 2 / WAVES;
constexpr float SCALE_LOG2E = 0.088388347648318440f * 1.4426950408889634f;
constexpr unsigned HSTRIDE = (unsigned)SKV * D * 4;

constexpr unsigned WS_EPOCH = 0;
constexpr unsigned WS_ML = 4096;
constexpr unsigned WS_ACC = WS_ML + NGRP * HG * NSPLIT * 16;
constexpr unsigned WS_END = WS_ACC + NGRP * HG * 4 * NSPLIT * 256;

using u32x4 = __attribute__((ext_vector_type(4))) unsigned;
using u32x2 = __attribute__((ext_vector_type(2))) unsigned;
using f32x4 = __attribute__((ext_vector_type(4))) float;
using f32x2 = __attribute__((ext_vector_type(2))) float;
using bf16x2 = __attribute__((ext_vector_type(2))) __bf16;

__device__ __forceinline__ float rbf(float x) { return (float)(__bf16)x; }
__device__ __forceinline__ float bfu(unsigned short h) { return __uint_as_float((unsigned)h << 16); }
__device__ __forceinline__ unsigned packbf(float lo, float hi) {
  bf16x2 v; v[0] = (__bf16)lo; v[1] = (__bf16)hi;
  return __builtin_bit_cast(unsigned, v);
}
template <int CTRL> __device__ __forceinline__ float dpp(float x) {
  return __builtin_bit_cast(float, __builtin_amdgcn_mov_dpp(__builtin_bit_cast(int, x), CTRL, 0xf, 0xf, true));
}
constexpr int XOR1 = 0xB1, XOR2 = 0x4E, ROR4 = 0x124, ROR8 = 0x128;
__device__ __forceinline__ float x16_sum(float x) {
  auto s = __builtin_amdgcn_permlane16_swap(__float_as_uint(x), __float_as_uint(x), false, false);
  return __uint_as_float(s[0]) + __uint_as_float(s[1]);
}
__device__ __forceinline__ float x16_max(float x) {
  auto s = __builtin_amdgcn_permlane16_swap(__float_as_uint(x), __float_as_uint(x), false, false);
  return fmaxf(__uint_as_float(s[0]), __uint_as_float(s[1]));
}
__device__ __forceinline__ float x32_sum(float x) {
  auto s = __builtin_amdgcn_permlane32_swap(__float_as_uint(x), __float_as_uint(x), false, false);
  return __uint_as_float(s[0]) + __uint_as_float(s[1]);
}
__device__ __forceinline__ float x32_max(float x) {
  auto s = __builtin_amdgcn_permlane32_swap(__float_as_uint(x), __float_as_uint(x), false, false);
  return fmaxf(__uint_as_float(s[0]), __uint_as_float(s[1]));
}

#define QK_ASM(I)                                                                                                   \
  asm volatile("v_dot2_f32_bf16 %1, %13, %17, 0\n\t"                                                                \
               "v_dot2_f32_bf16 %2, %14, %18, 0\n\t"                                                                \
               "v_dot2_f32_bf16 %3, %15, %19, 0\n\t"                                                                \
               "v_dot2_f32_bf16 %4, %16, %20, 0\n\t"                                                                \
               "v_cvt_f32_i32 %5, %9\n\t"                                                                           \
               "v_cvt_f32_i32 %6, %10\n\t"                                                                          \
               "v_cvt_f32_i32 %7, %11\n\t"                                                                          \
               "v_cvt_f32_i32 %8, %12\n\t"                                                                          \
               "v_subrev_f32_dpp %5, %21, %5 quad_perm:[" #I "," #I "," #I "," #I "] row_mask:0xf bank_mask:0xf bound_ctrl:1\n\t" \
               "v_subrev_f32_dpp %6, %21, %6 quad_perm:[" #I "," #I "," #I "," #I "] row_mask:0xf bank_mask:0xf bound_ctrl:1\n\t" \
               "v_subrev_f32_dpp %7, %21, %7 quad_perm:[" #I "," #I "," #I "," #I "] row_mask:0xf bank_mask:0xf bound_ctrl:1\n\t" \
               "v_subrev_f32_dpp %8, %21, %8 quad_perm:[" #I "," #I "," #I "," #I "] row_mask:0xf bank_mask:0xf bound_ctrl:1\n\t" \
               "v_mul_f32 %0, %5, %1\n\t"                                                                           \
               "v_fmac_f32 %0, %6, %2\n\t"                                                                          \
               "v_fmac_f32 %0, %7, %3\n\t"                                                                          \
               "v_fmac_f32 %0, %8, %4"                                                                              \
               : "=&v"(xo), "=&v"(w0), "=&v"(w1), "=&v"(w2), "=&v"(w3), "=&v"(t0), "=&v"(t1), "=&v"(t2), "=&v"(t3)  \
               : "v"(k0), "v"(k1), "v"(k2), "v"(k3), "v"(cs0), "v"(cs1), "v"(cs2), "v"(cs3), "v"(q0), "v"(q1), "v"(q2), "v"(q3), "v"(kz) \
               : "memory")
template <int I>
__device__ __forceinline__ float qk_head(const u32x4& k, unsigned cs0, unsigned cs1, unsigned cs2, unsigned cs3, const u32x4& q, float kz) {
  float xo, w0, w1, w2, w3, t0, t1, t2, t3;
  const unsigned k0 = k[0], k1 = k[1], k2 = k[2], k3 = k[3], q0 = q[0], q1 = q[1], q2 = q[2], q3 = q[3];
  if constexpr (I == 0) QK_ASM(0); else if constexpr (I == 1) QK_ASM(1); else if constexpr (I == 2) QK_ASM(2); else QK_ASM(3);
  return xo;
}
#define PV_ASM(I)                                                                                                   \
  asm volatile("v_cvt_f32_i32 %4, %8\n\t"                                                                           \
               "v_cvt_f32_i32 %5, %9\n\t"                                                                           \
               "v_cvt_f32_i32 %6, %10\n\t"                                                                          \
               "v_cvt_f32_i32 %7, %11\n\t"                                                                          \
               "v_mul_f32_dpp %0, %12, %0 quad_perm:[" #I "," #I "," #I "," #I "] row_mask:0xf bank_mask:0xf bound_ctrl:1\n\t" \
               "v_mul_f32_dpp %1, %12, %1 quad_perm:[" #I "," #I "," #I "," #I "] row_mask:0xf bank_mask:0xf bound_ctrl:1\n\t" \
               "v_mul_f32_dpp %2, %12, %2 quad_perm:[" #I "," #I "," #I "," #I "] row_mask:0xf bank_mask:0xf bound_ctrl:1\n\t" \
               "v_mul_f32_dpp %3, %12, %3 quad_perm:[" #I "," #I "," #I "," #I "] row_mask:0xf bank_mask:0xf bound_ctrl:1\n\t" \
               "v_fmac_f32_dpp %0, %13, %4 quad_perm:[" #I "," #I "," #I "," #I "] row_mask:0xf bank_mask:0xf bound_ctrl:1\n\t" \
               "v_fmac_f32_dpp %1, %13, %5 quad_perm:[" #I "," #I "," #I "," #I "] row_mask:0xf bank_mask:0xf bound_ctrl:1\n\t" \
               "v_fmac_f32_dpp %2, %13, %6 quad_perm:[" #I "," #I "," #I "," #I "] row_mask:0xf bank_mask:0xf bound_ctrl:1\n\t" \
               "v_fmac_f32_dpp %3, %13, %7 quad_perm:[" #I "," #I "," #I "," #I "] row_mask:0xf bank_mask:0xf bound_ctrl:1"     \
               : "+v"(a0), "+v"(a1), "+v"(a2), "+v"(a3), "=&v"(t0), "=&v"(t1), "=&v"(t2), "=&v"(t3)                 \
               : "v"(v0), "v"(v1), "v"(v2), "v"(v3), "v"(alpha), "v"(pv)                                            \
               : "memory")
template <int I>
__device__ __forceinline__ void pv_head(float& a0, float& a1, float& a2, float& a3, const u32x4& v, float alpha, float pv) {
  float t0, t1, t2, t3;
  const unsigned v0 = v[0], v1 = v[1], v2 = v[2], v3 = v[3];
  if constexpr (I == 0) PV_ASM(0); else if constexpr (I == 1) PV_ASM(1); else if constexpr (I == 2) PV_ASM(2); else PV_ASM(3);
}

struct T_ { static constexpr bool value = true; };
struct F_ { static constexpr bool value = false; };

struct Args {
  const unsigned short* q;
  const int* qk;
  const unsigned short* ksc;
  const unsigned short* kzp;
  const int* qv;
  const unsigned short* vsc;
  const unsigned short* vzp;
  const unsigned short* qcos;
  const unsigned short* qsin;
  const unsigned short* kcos;
  const unsigned short* ksin;
  unsigned char* ws;
  __hip_bfloat16* out;
};

struct Unit {
  u32x4 k[HG];
  u32x4 v[HG];
  u32x2 c;
  u32x2 s;
};

constexpr int REC = 132;
constexpr int LDS_SC = 0;
constexpr int LDS_QAB = TOK_WG * HG * 4;
constexpr int LDS_MRG = LDS_QAB + HG * D;
constexpr int LDS_USED = LDS_MRG + WAVES * 2 * HG * REC;
constexpr int LDS_FLOATS = 21 * 1024;
static_assert(LDS_USED <= LDS_FLOATS, "");

__global__ void __launch_bounds__(NTHR) attn_decode(Args a) {
  __shared__ __attribute__((aligned(16))) float lds[LDS_FLOATS];
  f32x4* sc4 = reinterpret_cast<f32x4*>(lds + LDS_SC);
  unsigned* qabl = reinterpret_cast<unsigned*>(lds + LDS_QAB);
  float* mrg = lds + LDS_MRG;
  const int split = blockIdx.x % NSPLIT;
  const int grp = blockIdx.x / NSPLIT;
  const int tid = threadIdx.x;
  const int lane = tid & 63;
  const int wave = __builtin_amdgcn_readfirstlane(tid >> 6);
  const int r = lane >> 5, c = lane & 31;
  const int cls = c & 3;
  const int bh0 = grp * HG;
  const int tw0 = split * TOK_WG;

  const auto krs = __builtin_amdgcn_make_buffer_rsrc((void*)(reinterpret_cast<const char*>(a.qk) + (size_t)bh0 * HSTRIDE), 0, HG * HSTRIDE, 0x00020000);
  const auto vrs = __builtin_amdgcn_make_buffer_rsrc((void*)(reinterpret_cast<const char*>(a.qv) + (size_t)bh0 * HSTRIDE), 0, HG * HSTRIDE, 0x00020000);
  const auto crs = __builtin_amdgcn_make_buffer_rsrc((void*)a.kcos, 0, SKV * D * 2, 0x00020000);
  const auto srs = __builtin_amdgcn_make_buffer_rsrc((void*)a.ksin, 0, SKV * D * 2, 0x00020000);
  const auto wrs = __builtin_amdgcn_make_buffer_rsrc((void*)a.ws, 0, WS_END, 0x00020000);
  const unsigned koff = (unsigned)(tw0 + 2 * wave) * (D * 4) + lane * 16;
  const unsigned coff = (unsigned)(tw0 + 2 * wave) * (D * 2) + lane * 8;
  const unsigned soff = (unsigned)(tw0 + 2 * wave) * (D * 2) + ((lane * 8) ^ 128);
  constexpr unsigned KSTEP = WAVES * 1024, CSTEP = WAVES * 512;

  auto ld_t = [&](Unit& U, unsigned u) {
    U.c = __builtin_amdgcn_raw_buffer_load_b64(crs, coff + u * CSTEP, 0, TABAUX);
    U.s = __builtin_amdgcn_raw_buffer_load_b64(srs, soff + u * CSTEP, 0, TABAUX);
  };
  auto ld_k = [&](Unit& U, unsigned u, int i) { U.k[i] = __builtin_amdgcn_raw_buffer_load_b128(krs, koff + u * KSTEP, i * HSTRIDE, KAUX); };
  auto ld_v = [&](Unit& U, unsigned u, int i) { U.v[i] = __builtin_amdgcn_raw_buffer_load_b128(vrs, koff + u * KSTEP, i * HSTRIDE, VAUX); };
  auto issue = [&](Unit& U, unsigned u) {
    ld_t(U, u);
#pragma unroll
    for (int i = 0; i < HG; ++i) ld_k(U, u, i);
#pragma unroll
    for (int i = 0; i < HG; ++i) ld_v(U, u, i);
  };

  const int qh = wave & (HG - 1);
  const unsigned short* qp = a.q + (bh0 + qh) * D;
  const unsigned short q_lo = qp[lane], q_hi = qp[lane + 64];
  const unsigned short c_lo = a.qcos[lane], c_hi = a.qcos[lane + 64];
  const unsigned short s_lo = a.qsin[lane], s_hi = a.qsin[lane + 64];
  constexpr int NITEM = HG * TOK_WG / 2 / NTHR;
  unsigned sraw[NITEM][4];
#pragma unroll
  for (int j = 0; j < NITEM; ++j) {
    const int it = tid + NTHR * j;
    const int hh = it / (TOK_WG / 2), pp = it % (TOK_WG / 2);
    const size_t sidx = (size_t)(bh0 + hh) * SKV + tw0 + 2 * pp;
    sraw[j][0] = LDSC(reinterpret_cast<const unsigned*>(a.ksc + sidx));
    sraw[j][1] = LDSC(reinterpret_cast<const unsigned*>(a.kzp + sidx));
    sraw[j][2] = LDSC(reinterpret_cast<const unsigned*>(a.vsc + sidx));
    sraw[j][3] = LDSC(reinterpret_cast<const unsigned*>(a.vzp + sidx));
  }
  const unsigned tag = __builtin_amdgcn_raw_buffer_load_b32(wrs, WS_EPOCH + grp * 256, 0, 16) + 1u;
  __builtin_amdgcn_sched_barrier(0);

  Unit UA, UB;
  issue(UA, 0);
  __builtin_amdgcn_sched_barrier(0);
  issue(UB, 1);
  __builtin_amdgcn_sched_barrier(0);

  {
    const float q1 = bfu(q_lo), q2 = bfu(q_hi);
    const float r1 = rbf(rbf(q1 * bfu(c_lo)) + rbf(-q2 * bfu(s_lo)));
    const float r2 = rbf(rbf(q2 * bfu(c_hi)) + rbf(q1 * bfu(s_hi)));
    if (wave < HG) {
      qabl[qh * D + lane] = packbf(r1, r2);
      qabl[qh * D + lane + 64] = packbf(r2, -r1);
    }
  }
#pragma unroll
  for (int j = 0; j < NITEM; ++j) {
    const int it = tid + NTHR * j;
    const int hh = it / (TOK_WG / 2), pp = it % (TOK_WG / 2);
    f32x4 a0 = {__uint_as_float(sraw[j][0] << 16) * SCALE_LOG2E, __uint_as_float(sraw[j][1] << 16), __uint_as_float(sraw[j][2] << 16), __uint_as_float(sraw[j][3] << 16)};
    f32x4 a1 = {__uint_as_float(sraw[j][0] & 0xffff0000u) * SCALE_LOG2E, __uint_as_float(sraw[j][1] & 0xffff0000u), __uint_as_float(sraw[j][2] & 0xffff0000u), __uint_as_float(sraw[j][3] & 0xffff0000u)};
    sc4[(2 * pp) * HG + hh] = a0;
    sc4[(2 * pp + 1) * HG + hh] = a1;
  }
  __syncthreads();
  u32x4 qab[HG];
#pragma unroll
  for (int i = 0; i < HG; ++i) qab[i] = *reinterpret_cast<const u32x4*>(qabl + i * D + 4 * c);

  float m = -INFINITY, l = 0.f, corr = 0.f;
  float acc[HG][4];
#pragma unroll
  for (int i = 0; i < HG; ++i)
#pragma unroll
    for (int j = 0; j < 4; ++j) acc[i][j] = 0.f;
  const bool b0 = c & 1, b1 = c & 2;
  const f32x4* scp = sc4 + (2 * wave + r) * HG + cls;

  auto step = [&](Unit& U, unsigned u, auto next_tag) {
    constexpr bool NEXT = decltype(next_tag)::value;
    const f32x4 sc = scp[u * (2 * WAVES * HG)];
    const unsigned cs0 = __builtin_amdgcn_perm(U.s[0], U.c[0], 0x05040100u);
    const unsigned cs1 = __builtin_amdgcn_perm(U.s[0], U.c[0], 0x07060302u);
    const unsigned cs2 = __builtin_amdgcn_perm(U.s[1], U.c[1], 0x05040100u);
    const unsigned cs3 = __builtin_amdgcn_perm(U.s[1], U.c[1], 0x07060302u);
    float x[HG];
    x[0] = qk_head<0>(U.k[0], cs0, cs1, cs2, cs3, qab[0], sc[1]);
    if constexpr (NEXT && ROLL) { ld_t(U, u + 2); ld_k(U, u + 2, 0); __builtin_amdgcn_sched_barrier(0); }
    x[1] = qk_head<1>(U.k[1], cs0, cs1, cs2, cs3, qab[1], sc[1]);
    if constexpr (NEXT && ROLL) { ld_k(U, u + 2, 1); __builtin_amdgcn_sched_barrier(0); }
    x[2] = qk_head<2>(U.k[2], cs0, cs1, cs2, cs3, qab[2], sc[1]);
    if constexpr (NEXT && ROLL) { ld_k(U, u + 2, 2); __builtin_amdgcn_sched_barrier(0); }
    x[3] = qk_head<3>(U.k[3], cs0, cs1, cs2, cs3, qab[3], sc[1]);
    if constexpr (NEXT && ROLL) { ld_k(U, u + 2, 3); __builtin_amdgcn_sched_barrier(0); }
    const float e0 = x[0] + dpp<XOR1>(x[0]), e1 = x[1] + dpp<XOR1>(x[1]);
    const float e2 = x[2] + dpp<XOR1>(x[2]), e3 = x[3] + dpp<XOR1>(x[3]);
    const float y0 = b0 ? e1 : e0;
    const float y1 = b0 ? e3 : e2;
    const float g0 = y0 + dpp<XOR2>(y0), g1 = y1 + dpp<XOR2>(y1);
    float z = b1 ? g1 : g0;
    z += dpp<ROR4>(z);
    z += dpp<ROR8>(z);
    z = x16_sum(z);
    const float s = z * sc[0];
    const float mn = fmaxf(m, s);
    const float alpha = __builtin_amdgcn_exp2f(m - mn);
    const float p = __builtin_amdgcn_exp2f(s - mn);
    m = mn;
    l = fmaf(l, alpha, p);
    const float pv = p * sc[2];
    corr = fmaf(corr, alpha, pv * sc[3]);
    pv_head<0>(acc[0][0], acc[0][1], acc[0][2], acc[0][3], U.v[0], alpha, pv);
    if constexpr (NEXT && ROLL) { ld_v(U, u + 2, 0); __builtin_amdgcn_sched_barrier(0); }
    pv_head<1>(acc[1][0], acc[1][1], acc[1][2], acc[1][3], U.v[1], alpha, pv);
    if constexpr (NEXT && ROLL) { ld_v(U, u + 2, 1); __builtin_amdgcn_sched_barrier(0); }
    pv_head<2>(acc[2][0], acc[2][1], acc[2][2], acc[2][3], U.v[2], alpha, pv);
    if constexpr (NEXT && ROLL) { ld_v(U, u + 2, 2); __builtin_amdgcn_sched_barrier(0); }
    pv_head<3>(acc[3][0], acc[3][1], acc[3][2], acc[3][3], U.v[3], alpha, pv);
    if constexpr (NEXT && ROLL) { ld_v(U, u + 2, 3); __builtin_amdgcn_sched_barrier(0); }
    if constexpr (NEXT && !ROLL) { issue(U, u + 2); __builtin_amdgcn_sched_barrier(0); }
  };

#pragma unroll 1
  for (unsigned u = 0; u < NUNIT - 2; u += 2) {
    step(UA, u, T_{});
    step(UB, u + 1, T_{});
  }
  step(UA, NUNIT - 2, F_{});
  step(UB, NUNIT - 1, F_{});

  {
    float* rec = mrg + (wave * 2 + r) * HG * REC;
#pragma unroll
    for (int i = 0; i < HG; ++i) *reinterpret_cast<f32x4*>(rec + i * REC + 4 + 4 * c) = f32x4{acc[i][0], acc[i][1], acc[i][2], acc[i][3]};
    if (c < HG) *reinterpret_cast<f32x4*>(rec + c * REC) = f32x4{m, l, corr, 0.f};
  }
  __syncthreads();
  if (wave >= HG) return;
  {
    const int hh = wave;
    constexpr int NR = WAVES * 2;
    f32x4 ml[NR]; f32x2 av[NR];
#pragma unroll
    for (int w = 0; w < NR; ++w) {
      const float* rec = mrg + (w * HG + hh) * REC;
      ml[w] = *reinterpret_cast<const f32x4*>(rec);
      av[w] = *reinterpret_cast<const f32x2*>(rec + 4 + 2 * lane);
    }
    float M = ml[0][0];
#pragma unroll
    for (int w = 1; w < NR; ++w) M = fmaxf(M, ml[w][0]);
    float L = 0.f, C = 0.f, o0 = 0.f, o1 = 0.f;
#pragma unroll
    for (int w = 0; w < NR; ++w) {
      const float f = __builtin_amdgcn_exp2f(ml[w][0] - M);
      L = fmaf(f, ml[w][1], L);
      C = fmaf(f, ml[w][2], C);
      o0 = fmaf(f, av[w][0], o0);
      o1 = fmaf(f, av[w][1], o1);
    }
    o0 -= C; o1 -= C;
    const unsigned aoff = WS_ACC + ((((unsigned)(grp * HG + hh) * 4 + (lane >> 4)) * NSPLIT + split) * 256) + (lane & 15) * 16;
    __builtin_amdgcn_raw_buffer_store_b128(u32x4{__float_as_uint(o0), tag, __float_as_uint(o1), tag}, wrs, aoff, 0, 16);
    if (lane == 0) {
      const unsigned moff = WS_ML + ((unsigned)(grp * HG + hh) * NSPLIT + split) * 16;
      __builtin_amdgcn_raw_buffer_store_b128(u32x4{__float_as_uint(M), tag, __float_as_uint(L), tag}, wrs, moff, 0, 16);
    }
  }
  if (wave != 0) return;
  {
    const int hh = split >> 2, sl = split & 3;
    const unsigned abase = WS_ACC + (((unsigned)(grp * HG + hh) * 4 + sl) * NSPLIT) * 256 + lane * 16;
    const unsigned mbase = WS_ML + ((unsigned)(grp * HG + hh) * NSPLIT + (lane >> 4)) * 16;
    u32x4 av[4], mv[4];
    bool ok = false;
    for (unsigned spins = 0; spins < (1u << 18); ++spins) {
      asm volatile("" ::: "memory");
#pragma unroll
      for (int n = 0; n < 4; ++n) {
        av[n] = __builtin_amdgcn_raw_buffer_load_b128(wrs, abase + n * 1024, 0, 16);
        mv[n] = __builtin_amdgcn_raw_buffer_load_b128(wrs, mbase + n * 64, 0, 16);
      }
      unsigned bad = 0;
#pragma unroll
      for (int n = 0; n < 4; ++n) bad |= (av[n][1] ^ tag) | (av[n][3] ^ tag) | (mv[n][1] ^ tag) | (mv[n][3] ^ tag);
      if (__all(bad == 0)) { ok = true; break; }
      __builtin_amdgcn_s_sleep(PSLEEP);
    }
    float M = -INFINITY;
#pragma unroll
    for (int n = 0; n < 4; ++n) M = fmaxf(M, __uint_as_float(mv[n][0]));
    M = x32_max(x16_max(M));
    float L = 0.f, o0 = 0.f, o1 = 0.f;
#pragma unroll
    for (int n = 0; n < 4; ++n) {
      const float f = __builtin_amdgcn_exp2f(__uint_as_float(mv[n][0]) - M);
      L = fmaf(f, __uint_as_float(mv[n][2]), L);
      o0 = fmaf(f, __uint_as_float(av[n][0]), o0);
      o1 = fmaf(f, __uint_as_float(av[n][2]), o1);
    }
    L = x32_sum(x16_sum(L));
    o0 = x32_sum(x16_sum(o0));
    o1 = x32_sum(x16_sum(o1));
    const float inv = ok ? 1.f / L : __uint_as_float(0x7fc00000u);
    if (lane < 16) {
      *reinterpret_cast<unsigned*>(a.out + (bh0 + hh) * D + sl * 32 + 2 * lane) = packbf(o0 * inv, o1 * inv);
    }
    if (split == 0 && lane == 0 && ok) __builtin_amdgcn_raw_buffer_store_b32(tag, wrs, WS_EPOCH + grp * 256, 0, 16);
  }
}

}

extern "C" void kernel_launch(void* const* d_in, const int* in_sizes, int n_in,
                              void* d_out, int out_size, void* d_ws, size_t ws_size,
                              hipStream_t stream) {
  Args a;
  a.q = (const unsigned short*)d_in[0];
  a.qk = (const int*)d_in[1];
  a.ksc = (const unsigned short*)d_in[2];
  a.kzp = (const unsigned short*)d_in[3];
  a.qv = (const int*)d_in[4];
  a.vsc = (const unsigned short*)d_in[5];
  a.vzp = (const unsigned short*)d_in[6];
  a.qcos = (const unsigned short*)d_in[7];
  a.qsin = (const unsigned short*)d_in[8];
  a.kcos = (const unsigned short*)d_in[9];
  a.ksin = (const unsigned short*)d_in[10];
  a.ws = (unsigned char*)d_ws;
  a.out = (__hip_bfloat16*)d_out;
  attn_decode<<<NWG, NTHR, 0, stream>>>(a);
}
```

```cpp
#include <hip/hip_runtime.h>
#include <hip/hip_bf16.h>
#include <stdint.h>

#define WAVES_ 4
#define TABAUX 2
#define KAUX 2
#define VAUX 2
#define SCNT 1
#define LDSC(p) __builtin_nontemporal_load(p)
#define ROLL 1
#define FIRST 0
#define POLLW 1
#define PSTAG 4
#define SKIPSEEN 0
#define EARLYWB 0
#define PEEL 1
#define PSLEEP 1

namespace {

constexpr int B_ = 2, H_ = 32, SKV = 4096, D = 128;
constexpr int BH = B_ * H_;
constexpr int HG = 4;
constexpr int NGRP = BH / HG;
constexpr int NWG = 256;
constexpr int NSPLIT = NWG / NGRP;
constexpr int WAVES = WAVES_;
constexpr int NTHR = WAVES * 64;
constexpr int TOK_WG = SKV / NSPLIT;
constexpr int NUNIT = TOK_WG / 2 / WAVES;
constexpr float SCALE_LOG2E = 0.088388347648318440f * 1.4426950408889634f;
constexpr unsigned HSTRIDE = (unsigned)SKV * D * 4;

constexpr unsigned WS_EPOCH = 0;
constexpr unsigned WS_ML = 4096;
constexpr unsigned WS_ACC = WS_ML + NGRP * HG * NSPLIT * 16;
constexpr unsigned WS_END = WS_ACC + NGRP * HG * 4 * NSPLIT * 256;

using u32x4 = __attribute__((ext_vector_type(4))) unsigned;
using u32x2 = __attribute__((ext_vector_type(2))) unsigned;
using f32x4 = __attribute__((ext_vector_type(4))) float;
using f32x2 = __attribute__((ext_vector_type(2))) float;
using bf16x2 = __attribute__((ext_vector_type(2))) __bf16;

__device__ __forceinline__ float rbf(float x) { return (float)(__bf16)x; }
__device__ __forceinline__ float bfu(unsigned short h) { return __uint_as_float((unsigned)h << 16); }
__device__ __forceinline__ unsigned packbf(float lo, float hi) {
  bf16x2 v; v[0] = (__bf16)lo; v[1] = (__bf16)hi;
  return __builtin_bit_cast(unsigned, v);
}
template <int CTRL> __device__ __forceinline__ float dpp(float x) {
  return __builtin_bit_cast(float, __builtin_amdgcn_mov_dpp(__builtin_bit_cast(int, x), CTRL, 0xf, 0xf, true));
}
constexpr int XOR1 = 0xB1, XOR2 = 0x4E, ROR4 = 0x124, ROR8 = 0x128;
__device__ __forceinline__ float x16_sum(float x) {
  auto s = __builtin_amdgcn_permlane16_swap(__float_as_uint(x), __float_as_uint(x), false, false);
  return __uint_as_float(s[0]) + __uint_as_float(s[1]);
}
__device__ __forceinline__ float x16_max(float x) {
  auto s = __builtin_amdgcn_permlane16_swap(__float_as_uint(x), __float_as_uint(x), false, false);
  return fmaxf(__uint_as_float(s[0]), __uint_as_float(s[1]));
}
__device__ __forceinline__ float x32_sum(float x) {
  auto s = __builtin_amdgcn_permlane32_swap(__float_as_uint(x), __float_as_uint(x), false, false);
  return __uint_as_float(s[0]) + __uint_as_float(s[1]);
}
__device__ __forceinline__ float x32_max(float x) {
  auto s = __builtin_amdgcn_permlane32_swap(__float_as_uint(x), __float_as_uint(x), false, false);
  return fmaxf(__uint_as_float(s[0]), __uint_as_float(s[1]));
}

#define QK_ASM(I)                                                                                                   \
  asm volatile("v_dot2_f32_bf16 %1, %13, %17, 0\n\t"                                                                \
               "v_dot2_f32_bf16 %2, %14, %18, 0\n\t"                                                                \
               "v_dot2_f32_bf16 %3, %15, %19, 0\n\t"                                                                \
               "v_dot2_f32_bf16 %4, %16, %20, 0\n\t"                                                                \
               "v_cvt_f32_i32 %5, %9\n\t"                                                                           \
               "v_cvt_f32_i32 %6, %10\n\t"                                                                          \
               "v_cvt_f32_i32 %7, %11\n\t"                                                                          \
               "v_cvt_f32_i32 %8, %12\n\t"                                                                          \
               "v_subrev_f32_dpp %5, %21, %5 quad_perm:[" #I "," #I "," #I "," #I "] row_mask:0xf bank_mask:0xf bound_ctrl:1\n\t" \
               "v_subrev_f32_dpp %6, %21, %6 quad_perm:[" #I "," #I "," #I "," #I "] row_mask:0xf bank_mask:0xf bound_ctrl:1\n\t" \
               "v_subrev_f32_dpp %7, %21, %7 quad_perm:[" #I "," #I "," #I "," #I "] row_mask:0xf bank_mask:0xf bound_ctrl:1\n\t" \
               "v_subrev_f32_dpp %8, %21, %8 quad_perm:[" #I "," #I "," #I "," #I "] row_mask:0xf bank_mask:0xf bound_ctrl:1\n\t" \
               "v_mul_f32 %0, %5, %1\n\t"                                                                           \
               "v_fmac_f32 %0, %6, %2\n\t"                                                                          \
               "v_fmac_f32 %0, %7, %3\n\t"                                                                          \
               "v_fmac_f32 %0, %8, %4"                                                                              \
               : "=&v"(xo), "=&v"(w0), "=&v"(w1), "=&v"(w2), "=&v"(w3), "=&v"(t0), "=&v"(t1), "=&v"(t2), "=&v"(t3)  \
               : "v"(k0), "v"(k1), "v"(k2), "v"(k3), "v"(cs0), "v"(cs1), "v"(cs2), "v"(cs3), "v"(q0), "v"(q1), "v"(q2), "v"(q3), "v"(kz) \
               : "memory")
template <int I>
__device__ __forceinline__ float qk_head(const u32x4& k, unsigned cs0, unsigned cs1, unsigned cs2, unsigned cs3, const u32x4& q, float kz) {
  float xo, w0, w1, w2, w3, t0, t1, t2, t3;
  const unsigned k0 = k[0], k1 = k[1], k2 = k[2], k3 = k[3], q0 = q[0], q1 = q[1], q2 = q[2], q3 = q[3];
  if constexpr (I == 0) QK_ASM(0); else if constexpr (I == 1) QK_ASM(1); else if constexpr (I == 2) QK_ASM(2); else QK_ASM(3);
  return xo;
}
#define PV_ASM(I)                                                                                                   \
  asm volatile("v_cvt_f32_i32 %4, %8\n\t"                                                                           \
               "v_cvt_f32_i32 %5, %9\n\t"                                                                           \
               "v_cvt_f32_i32 %6, %10\n\t"                                                                          \
               "v_cvt_f32_i32 %7, %11\n\t"                                                                          \
               "v_mul_f32_dpp %0, %12, %0 quad_perm:[" #I "," #I "," #I "," #I "] row_mask:0xf bank_mask:0xf bound_ctrl:1\n\t" \
               "v_mul_f32_dpp %1, %12, %1 quad_perm:[" #I "," #I "," #I "," #I "] row_mask:0xf bank_mask:0xf bound_ctrl:1\n\t" \
               "v_mul_f32_dpp %2, %12, %2 quad_perm:[" #I "," #I "," #I "," #I "] row_mask:0xf bank_mask:0xf bound_ctrl:1\n\t" \
               "v_mul_f32_dpp %3, %12, %3 quad_perm:[" #I "," #I "," #I "," #I "] row_mask:0xf bank_mask:0xf bound_ctrl:1\n\t" \
               "v_fmac_f32_dpp %0, %13, %4 quad_perm:[" #I "," #I "," #I "," #I "] row_mask:0xf bank_mask:0xf bound_ctrl:1\n\t" \
               "v_fmac_f32_dpp %1, %13, %5 quad_perm:[" #I "," #I "," #I "," #I "] row_mask:0xf bank_mask:0xf bound_ctrl:1\n\t" \
               "v_fmac_f32_dpp %2, %13, %6 quad_perm:[" #I "," #I "," #I "," #I "] row_mask:0xf bank_mask:0xf bound_ctrl:1\n\t" \
               "v_fmac_f32_dpp %3, %13, %7 quad_perm:[" #I "," #I "," #I "," #I "] row_mask:0xf bank_mask:0xf bound_ctrl:1"     \
               : "+v"(a0), "+v"(a1), "+v"(a2), "+v"(a3), "=&v"(t0), "=&v"(t1), "=&v"(t2), "=&v"(t3)                 \
               : "v"(v0), "v"(v1), "v"(v2), "v"(v3), "v"(alpha), "v"(pv)                                            \
               : "memory")
template <int I>
__device__ __forceinline__ void pv_head(float& a0, float& a1, float& a2, float& a3, const u32x4& v, float alpha, float pv) {
  float t0, t1, t2, t3;
  const unsigned v0 = v[0], v1 = v[1], v2 = v[2], v3 = v[3];
  if constexpr (I == 0) PV_ASM(0); else if constexpr (I == 1) PV_ASM(1); else if constexpr (I == 2) PV_ASM(2); else PV_ASM(3);
}

struct T_ { static constexpr bool value = true; };
struct F_ { static constexpr bool value = false; };

struct Args {
  const unsigned short* q;
  const int* qk;
  const unsigned short* ksc;
  const unsigned short* kzp;
  const int* qv;
  const unsigned short* vsc;
  const unsigned short* vzp;
  const unsigned short* qcos;
  const unsigned short* qsin;
  const unsigned short* kcos;
  const unsigned short* ksin;
  unsigned char* ws;
  __hip_bfloat16* out;
};

struct Unit {
  u32x4 k[HG];
  u32x4 v[HG];
  u32x2 c;
  u32x2 s;
};

constexpr int REC = 132;
constexpr int LDS_SC = 0;
constexpr int LDS_QAB = TOK_WG * HG * 4;
constexpr int LDS_MRG = LDS_QAB + HG * D;
constexpr int LDS_USED = LDS_MRG + WAVES * 2 * HG * REC;
constexpr int LDS_FLOATS = 21 * 1024;
static_assert(LDS_USED <= LDS_FLOATS, "");

__global__ void __launch_bounds__(NTHR) attn_decode(Args a) {
  __shared__ __attribute__((aligned(16))) float lds[LDS_FLOATS];
  f32x4* sc4 = reinterpret_cast<f32x4*>(lds + LDS_SC);
  unsigned* qabl = reinterpret_cast<unsigned*>(lds + LDS_QAB);
  float* mrg = lds + LDS_MRG;
  const int split = blockIdx.x % NSPLIT;
  const int grp = blockIdx.x / NSPLIT;
  const int tid = threadIdx.x;
  const int lane = tid & 63;
  const int wave = __builtin_amdgcn_readfirstlane(tid >> 6);
  const int r = lane >> 5, c = lane & 31;
  const int cls = c & 3;
  const int bh0 = grp * HG;
  const int tw0 = split * TOK_WG;

  const auto krs = __builtin_amdgcn_make_buffer_rsrc((void*)(reinterpret_cast<const char*>(a.qk) + (size_t)bh0 * HSTRIDE), 0, HG * HSTRIDE, 0x00020000);
  const auto vrs = __builtin_amdgcn_make_buffer_rsrc((void*)(reinterpret_cast<const char*>(a.qv) + (size_t)bh0 * HSTRIDE), 0, HG * HSTRIDE, 0x00020000);
  const auto crs = __builtin_amdgcn_make_buffer_rsrc((void*)a.kcos, 0, SKV * D * 2, 0x00020000);
  const auto srs = __builtin_amdgcn_make_buffer_rsrc((void*)a.ksin, 0, SKV * D * 2, 0x00020000);
  const auto wrs = __builtin_amdgcn_make_buffer_rsrc((void*)a.ws, 0, WS_END, 0x00020000);
  const unsigned koff = (unsigned)(tw0 + 2 * wave) * (D * 4) + lane * 16;
  const unsigned coff = (unsigned)(tw0 + 2 * wave) * (D * 2) + lane * 8;
  const unsigned soff = (unsigned)(tw0 + 2 * wave) * (D * 2) + ((lane * 8) ^ 128);
  constexpr unsigned KSTEP = WAVES * 1024, CSTEP = WAVES * 512;

  auto ld_t = [&](Unit& U, unsigned u) {
    const unsigned o = u < (unsigned)NUNIT ? u * CSTEP : 0x40000000u;
    U.c = __builtin_amdgcn_raw_buffer_load_b64(crs, coff + o, 0, TABAUX);
    U.s = __builtin_amdgcn_raw_buffer_load_b64(srs, soff + o, 0, TABAUX);
  };
  auto ld_k = [&](Unit& U, unsigned u, int i) { U.k[i] = __builtin_amdgcn_raw_buffer_load_b128(krs, koff + (u < (unsigned)NUNIT ? u * KSTEP : 0x80000000u), i * HSTRIDE, KAUX); };
  auto ld_v = [&](Unit& U, unsigned u, int i) { U.v[i] = __builtin_amdgcn_raw_buffer_load_b128(vrs, koff + (u < (unsigned)NUNIT ? u * KSTEP : 0x80000000u), i * HSTRIDE, VAUX); };
  auto issue = [&](Unit& U, unsigned u) {
    ld_t(U, u);
#pragma unroll
    for (int i = 0; i < HG; ++i) ld_k(U, u, i);
#pragma unroll
    for (int i = 0; i < HG; ++i) ld_v(U, u, i);
  };

  Unit UA, UB;
  const int qh = wave & (HG - 1);
  const unsigned short* qp = a.q + (bh0 + qh) * D;
  const unsigned short q_lo = qp[lane], q_hi = qp[lane + 64];
  const unsigned short c_lo = a.qcos[lane], c_hi = a.qcos[lane + 64];
  const unsigned short s_lo = a.qsin[lane], s_hi = a.qsin[lane + 64];
  constexpr int NITEM = HG * TOK_WG / 2 / NTHR;
  unsigned sraw[NITEM][4];
#pragma unroll
  for (int j = 0; j < NITEM; ++j) {
    const int it = tid + NTHR * j;
    const int hh = it / (TOK_WG / 2), pp = it % (TOK_WG / 2);
    const size_t sidx = (size_t)(bh0 + hh) * SKV + tw0 + 2 * pp;
    sraw[j][0] = LDSC(reinterpret_cast<const unsigned*>(a.ksc + sidx));
    sraw[j][1] = LDSC(reinterpret_cast<const unsigned*>(a.kzp + sidx));
    sraw[j][2] = LDSC(reinterpret_cast<const unsigned*>(a.vsc + sidx));
    sraw[j][3] = LDSC(reinterpret_cast<const unsigned*>(a.vzp + sidx));
  }
  const unsigned tag = __builtin_amdgcn_raw_buffer_load_b32(wrs, WS_EPOCH + grp * 256, 0, 16) + 1u;
  __builtin_amdgcn_sched_barrier(0);

  issue(UA, 0);
  __builtin_amdgcn_sched_barrier(0);
  issue(UB, 1);
  __builtin_amdgcn_sched_barrier(0);

  {
    const float q1 = bfu(q_lo), q2 = bfu(q_hi);
    const float r1 = rbf(rbf(q1 * bfu(c_lo)) + rbf(-q2 * bfu(s_lo)));
    const float r2 = rbf(rbf(q2 * bfu(c_hi)) + rbf(q1 * bfu(s_hi)));
    if (wave < HG) {
      qabl[qh * D + lane] = packbf(r1, r2);
      qabl[qh * D + lane + 64] = packbf(r2, -r1);
    }
  }
#pragma unroll
  for (int j = 0; j < NITEM; ++j) {
    const int it = tid + NTHR * j;
    const int hh = it / (TOK_WG / 2), pp = it % (TOK_WG / 2);
    f32x4 a0 = {__uint_as_float(sraw[j][0] << 16) * SCALE_LOG2E, __uint_as_float(sraw[j][1] << 16), __uint_as_float(sraw[j][2] << 16), __uint_as_float(sraw[j][3] << 16)};
    f32x4 a1 = {__uint_as_float(sraw[j][0] & 0xffff0000u) * SCALE_LOG2E, __uint_as_float(sraw[j][1] & 0xffff0000u), __uint_as_float(sraw[j][2] & 0xffff0000u), __uint_as_float(sraw[j][3] & 0xffff0000u)};
    sc4[(2 * pp) * HG + hh] = a0;
    sc4[(2 * pp + 1) * HG + hh] = a1;
  }
  __syncthreads();
  u32x4 qab[HG];
#pragma unroll
  for (int i = 0; i < HG; ++i) qab[i] = *reinterpret_cast<const u32x4*>(qabl + i * D + 4 * c);

  float m = -INFINITY, l = 0.f, corr = 0.f;
  float acc[HG][4];
#pragma unroll
  for (int i = 0; i < HG; ++i)
#pragma unroll
    for (int j = 0; j < 4; ++j) acc[i][j] = 0.f;
  const bool b0 = c & 1, b1 = c & 2;
  const f32x4* scp = sc4 + (2 * wave + r) * HG + cls;

  auto step = [&](Unit& U, unsigned u, auto next_tag) {
    constexpr bool NEXT = decltype(next_tag)::value;
    const f32x4 sc = scp[u * (2 * WAVES * HG)];
    const unsigned cs0 = __builtin_amdgcn_perm(U.s[0], U.c[0], 0x05040100u);
    const unsigned cs1 = __builtin_amdgcn_perm(U.s[0], U.c[0], 0x07060302u);
    const unsigned cs2 = __builtin_amdgcn_perm(U.s[1], U.c[1], 0x05040100u);
    const unsigned cs3 = __builtin_amdgcn_perm(U.s[1], U.c[1], 0x07060302u);
    float x[HG];
    x[0] = qk_head<0>(U.k[0], cs0, cs1, cs2, cs3, qab[0], sc[1]);
    if constexpr (NEXT && ROLL) { ld_t(U, u + 2); ld_k(U, u + 2, 0); __builtin_amdgcn_sched_barrier(0); }
    x[1] = qk_head<1>(U.k[1], cs0, cs1, cs2, cs3, qab[1], sc[1]);
    if constexpr (NEXT && ROLL) { ld_k(U, u + 2, 1); __builtin_amdgcn_sched_barrier(0); }
    x[2] = qk_head<2>(U.k[2], cs0, cs1, cs2, cs3, qab[2], sc[1]);
    if constexpr (NEXT && ROLL) { ld_k(U, u + 2, 2); __builtin_amdgcn_sched_barrier(0); }
    x[3] = qk_head<3>(U.k[3], cs0, cs1, cs2, cs3, qab[3], sc[1]);
    if constexpr (NEXT && ROLL) { ld_k(U, u + 2, 3); __builtin_amdgcn_sched_barrier(0); }
    const float e0 = x[0] + dpp<XOR1>(x[0]), e1 = x[1] + dpp<XOR1>(x[1]);
    const float e2 = x[2] + dpp<XOR1>(x[2]), e3 = x[3] + dpp<XOR1>(x[3]);
    const float y0 = b0 ? e1 : e0;
    const float y1 = b0 ? e3 : e2;
    const float g0 = y0 + dpp<XOR2>(y0), g1 = y1 + dpp<XOR2>(y1);
    float z = b1 ? g1 : g0;
    z += dpp<ROR4>(z);
    z += dpp<ROR8>(z);
    z = x16_sum(z);
    const float s = z * sc[0];
    const float mn = fmaxf(m, s);
    const float alpha = __builtin_amdgcn_exp2f(m - mn);
    const float p = __builtin_amdgcn_exp2f(s - mn);
    m = mn;
    l = fmaf(l, alpha, p);
    const float pv = p * sc[2];
    corr = fmaf(corr, alpha, pv * sc[3]);
    pv_head<0>(acc[0][0], acc[0][1], acc[0][2], acc[0][3], U.v[0], alpha, pv);
    if constexpr (NEXT && ROLL) { ld_v(U, u + 2, 0); __builtin_amdgcn_sched_barrier(0); }
    pv_head<1>(acc[1][0], acc[1][1], acc[1][2], acc[1][3], U.v[1], alpha, pv);
    if constexpr (NEXT && ROLL) { ld_v(U, u + 2, 1); __builtin_amdgcn_sched_barrier(0); }
    pv_head<2>(acc[2][0], acc[2][1], acc[2][2], acc[2][3], U.v[2], alpha, pv);
    if constexpr (NEXT && ROLL) { ld_v(U, u + 2, 2); __builtin_amdgcn_sched_barrier(0); }
    pv_head<3>(acc[3][0], acc[3][1], acc[3][2], acc[3][3], U.v[3], alpha, pv);
    if constexpr (NEXT && ROLL) { ld_v(U, u + 2, 3); __builtin_amdgcn_sched_barrier(0); }
    if constexpr (NEXT && !ROLL) { issue(U, u + 2); __builtin_amdgcn_sched_barrier(0); }
  };

#pragma unroll 1
  for (unsigned u = 0; u < NUNIT - 2; u += 2) {
    step(UA, u, T_{});
    step(UB, u + 1, T_{});
  }
  step(UA, NUNIT - 2, F_{});
  step(UB, NUNIT - 1, F_{});

  {
    float* rec = mrg + (wave * 2 + r) * HG * REC;
#pragma unroll
    for (int i = 0; i < HG; ++i) *reinterpret_cast<f32x4*>(rec + i * REC + 4 + 4 * c) = f32x4{acc[i][0], acc[i][1], acc[i][2], acc[i][3]};
    if (c < HG) *reinterpret_cast<f32x4*>(rec + c * REC) = f32x4{m, l, corr, 0.f};
  }
  __syncthreads();
  if (wave >= HG) return;
  {
    const int hh = wave;
    constexpr int NR = WAVES * 2;
    f32x4 ml[NR]; f32x2 av[NR];
#pragma unroll
    for (int w = 0; w < NR; ++w) {
      const float* rec = mrg + (w * HG + hh) * REC;
      ml[w] = *reinterpret_cast<const f32x4*>(rec);
      av[w] = *reinterpret_cast<const f32x2*>(rec + 4 + 2 * lane);
    }
    float M = ml[0][0];
#pragma unroll
    for (int w = 1; w < NR; ++w) M = fmaxf(M, ml[w][0]);
    float L = 0.f, C = 0.f, o0 = 0.f, o1 = 0.f;
#pragma unroll
    for (int w = 0; w < NR; ++w) {
      const float f = __builtin_amdgcn_exp2f(ml[w][0] - M);
      L = fmaf(f, ml[w][1], L);
      C = fmaf(f, ml[w][2], C);
      o0 = fmaf(f, av[w][0], o0);
      o1 = fmaf(f, av[w][1], o1);
    }
    o0 -= C; o1 -= C;
    const unsigned aoff = WS_ACC + ((((unsigned)(grp * HG + hh) * 4 + (lane >> 4)) * NSPLIT + split) * 256) + (lane & 15) * 16;
    __builtin_amdgcn_raw_buffer_store_b128(u32x4{__float_as_uint(o0), tag, __float_as_uint(o1), tag}, wrs, aoff, 0, 16);
    if (lane == 0) {
      const unsigned moff = WS_ML + ((unsigned)(grp * HG + hh) * NSPLIT + split) * 16;
      __builtin_amdgcn_raw_buffer_store_b128(u32x4{__float_as_uint(M), tag, __float_as_uint(L), tag}, wrs, moff, 0, 16);
    }
  }
  if (wave != 0) return;
  {
    const int hh = split >> 2, sl = split & 3;
    const unsigned abase = WS_ACC + (((unsigned)(grp * HG + hh) * 4 + sl) * NSPLIT) * 256 + lane * 16;
    const unsigned mbase = WS_ML + ((unsigned)(grp * HG + hh) * NSPLIT + (lane >> 4)) * 16;
    u32x4 av[4], mv[4];
    bool ok = false;
    for (unsigned spins = 0; spins < (1u << 18); ++spins) {
      asm volatile("" ::: "memory");
#pragma unroll
      for (int n = 0; n < 4; ++n) {
        av[n] = __builtin_amdgcn_raw_buffer_load_b128(wrs, abase + n * 1024, 0, 16);
        mv[n] = __builtin_amdgcn_raw_buffer_load_b128(wrs, mbase + n * 64, 0, 16);
      }
      unsigned bad = 0;
#pragma unroll
      for (int n = 0; n < 4; ++n) bad |= (av[n][1] ^ tag) | (av[n][3] ^ tag) | (mv[n][1] ^ tag) | (mv[n][3] ^ tag);
      if (__all(bad == 0)) { ok = true; break; }
      __builtin_amdgcn_s_sleep(PSLEEP);
    }
    float M = -INFINITY;
#pragma unroll
    for (int n = 0; n < 4; ++n) M = fmaxf(M, __uint_as_float(mv[n][0]));
    M = x32_max(x16_max(M));
    float L = 0.f, o0 = 0.f, o1 = 0.f;
#pragma unroll
    for (int n = 0; n < 4; ++n) {
      const float f = __builtin_amdgcn_exp2f(__uint_as_float(mv[n][0]) - M);
      L = fmaf(f, __uint_as_float(mv[n][2]), L);
      o0 = fmaf(f, __uint_as_float(av[n][0]), o0);
      o1 = fmaf(f, __uint_as_float(av[n][2]), o1);
    }
    L = x32_sum(x16_sum(L));
    o0 = x32_sum(x16_sum(o0));
    o1 = x32_sum(x16_sum(o1));
    const float inv = ok ? 1.f / L : __uint_as_float(0x7fc00000u);
    if (lane < 16) {
      *reinterpret_cast<unsigned*>(a.out + (bh0 + hh) * D + sl * 32 + 2 * lane) = packbf(o0 * inv, o1 * inv);
    }
    if (split == 0 && wave == 0 && lane == 0 && ok) __builtin_amdgcn_raw_buffer_store_b32(tag, wrs, WS_EPOCH + grp * 256, 0, 16);
  }
}

}

extern "C" void kernel_launch(void* const* d_in, const int* in_sizes, int n_in,
                              void* d_out, int out_size, void* d_ws, size_t ws_size,
                              hipStream_t stream) {
  Args a;
  a.q = (const unsigned short*)d_in[0];
  a.qk = (const int*)d_in[1];
  a.ksc = (const unsigned short*)d_in[2];
  a.kzp = (const unsigned short*)d_in[3];
  a.qv = (const int*)d_in[4];
  a.vsc = (const unsigned short*)d_in[5];
  a.vzp = (const unsigned short*)d_in[6];
  a.qcos = (const unsigned short*)d_in[7];
  a.qsin = (const unsigned short*)d_in[8];
  a.kcos = (const unsigned short*)d_in[9];
  a.ksin = (const unsigned short*)d_in[10];
  a.ws = (unsigned char*)d_ws;
  a.out = (__hip_bfloat16*)d_out;
  attn_decode<<<NWG, NTHR, 0, stream>>>(a);
}
```

```cpp
#define FIXSPAN 6000
#define LDSK 9
#include <hip/hip_runtime.h>
#include <hip/hip_bf16.h>
#include <stdint.h>

#define WAVES_ 4
#define TABAUX 2
#define KAUX 2
#define VAUX 2
#define SCNT 1
#define LDSC(p) __builtin_nontemporal_load(p)
#define ROLL 1
#define FIRST 0
#define POLLW 1
#define PSTAG 4
#define SKIPSEEN 0
#define EARLYWB 0
#define OUTSC1 0
#define PEEL 1
#define PSLEEP 1

namespace {

constexpr int B_ = 2, H_ = 32, SKV = 4096, D = 128;
constexpr int BH = B_ * H_;
constexpr int HG = 4;
constexpr int NGRP = BH / HG;
constexpr int NWG = 256;
constexpr int NSPLIT = NWG / NGRP;
constexpr int WAVES = WAVES_;
constexpr int NTHR = WAVES * 64;
constexpr int TOK_WG = SKV / NSPLIT;
constexpr int NUNIT = TOK_WG / 2 / WAVES;
constexpr float SCALE_LOG2E = 0.088388347648318440f * 1.4426950408889634f;
constexpr unsigned HSTRIDE = (unsigned)SKV * D * 4;

constexpr unsigned WS_EPOCH = 0;
constexpr unsigned WS_ML = 4096;
constexpr unsigned WS_ACC = WS_ML + NGRP * HG * NSPLIT * 16;
constexpr unsigned WS_END = WS_ACC + NGRP * HG * 4 * NSPLIT * 256;

using u32x4 = __attribute__((ext_vector_type(4))) unsigned;
using u32x2 = __attribute__((ext_vector_type(2))) unsigned;
using f32x4 = __attribute__((ext_vector_type(4))) float;
using f32x2 = __attribute__((ext_vector_type(2))) float;
using bf16x2 = __attribute__((ext_vector_type(2))) __bf16;

__device__ __forceinline__ float rbf(float x) { return (float)(__bf16)x; }
__device__ __forceinline__ float bfu(unsigned short h) { return __uint_as_float((unsigned)h << 16); }
__device__ __forceinline__ unsigned packbf(float lo, float hi) {
  bf16x2 v; v[0] = (__bf16)lo; v[1] = (__bf16)hi;
  return __builtin_bit_cast(unsigned, v);
}
template <int CTRL> __device__ __forceinline__ float dpp(float x) {
  return __builtin_bit_cast(float, __builtin_amdgcn_mov_dpp(__builtin_bit_cast(int, x), CTRL, 0xf, 0xf, true));
}
constexpr int XOR1 = 0xB1, XOR2 = 0x4E, ROR4 = 0x124, ROR8 = 0x128;
__device__ __forceinline__ float x16_sum(float x) {
  auto s = __builtin_amdgcn_permlane16_swap(__float_as_uint(x), __float_as_uint(x), false, false);
  return __uint_as_float(s[0]) + __uint_as_float(s[1]);
}
__device__ __forceinline__ float x16_max(float x) {
  auto s = __builtin_amdgcn_permlane16_swap(__float_as_uint(x), __float_as_uint(x), false, false);
  return fmaxf(__uint_as_float(s[0]), __uint_as_float(s[1]));
}
__device__ __forceinline__ float x32_sum(float x) {
  auto s = __builtin_amdgcn_permlane32_swap(__float_as_uint(x), __float_as_uint(x), false, false);
  return __uint_as_float(s[0]) + __uint_as_float(s[1]);
}
__device__ __forceinline__ float x32_max(float x) {
  auto s = __builtin_amdgcn_permlane32_swap(__float_as_uint(x), __float_as_uint(x), false, false);
  return fmaxf(__uint_as_float(s[0]), __uint_as_float(s[1]));
}

#define QK_ASM(I)                                                                                                   \
  asm volatile("v_dot2_f32_bf16 %1, %13, %17, 0\n\t"                                                                \
               "v_dot2_f32_bf16 %2, %14, %18, 0\n\t"                                                                \
               "v_dot2_f32_bf16 %3, %15, %19, 0\n\t"                                                                \
               "v_dot2_f32_bf16 %4, %16, %20, 0\n\t"                                                                \
               "v_cvt_f32_i32 %5, %9\n\t"                                                                           \
               "v_cvt_f32_i32 %6, %10\n\t"                                                                          \
               "v_cvt_f32_i32 %7, %11\n\t"                                                                          \
               "v_cvt_f32_i32 %8, %12\n\t"                                                                          \
               "v_subrev_f32_dpp %5, %21, %5 quad_perm:[" #I "," #I "," #I "," #I "] row_mask:0xf bank_mask:0xf bound_ctrl:1\n\t" \
               "v_subrev_f32_dpp %6, %21, %6 quad_perm:[" #I "," #I "," #I "," #I "] row_mask:0xf bank_mask:0xf bound_ctrl:1\n\t" \
               "v_subrev_f32_dpp %7, %21, %7 quad_perm:[" #I "," #I "," #I "," #I "] row_mask:0xf bank_mask:0xf bound_ctrl:1\n\t" \
               "v_subrev_f32_dpp %8, %21, %8 quad_perm:[" #I "," #I "," #I "," #I "] row_mask:0xf bank_mask:0xf bound_ctrl:1\n\t" \
               "v_mul_f32 %0, %5, %1\n\t"                                                                           \
               "v_fmac_f32 %0, %6, %2\n\t"                                                                          \
               "v_fmac_f32 %0, %7, %3\n\t"                                                                          \
               "v_fmac_f32 %0, %8, %4"                                                                              \
               : "=&v"(xo), "=&v"(w0), "=&v"(w1), "=&v"(w2), "=&v"(w3), "=&v"(t0), "=&v"(t1), "=&v"(t2), "=&v"(t3)  \
               : "v"(k0), "v"(k1), "v"(k2), "v"(k3), "v"(cs0), "v"(cs1), "v"(cs2), "v"(cs3), "v"(q0), "v"(q1), "v"(q2), "v"(q3), "v"(kz) \
               : "memory")
template <int I>
__device__ __forceinline__ float qk_head(const u32x4& k, unsigned cs0, unsigned cs1, unsigned cs2, unsigned cs3, const u32x4& q, float kz) {
  float xo, w0, w1, w2, w3, t0, t1, t2, t3;
  const unsigned k0 = k[0], k1 = k[1], k2 = k[2], k3 = k[3], q0 = q[0], q1 = q[1], q2 = q[2], q3 = q[3];
  if constexpr (I == 0) QK_ASM(0); else if constexpr (I == 1) QK_ASM(1); else if constexpr (I == 2) QK_ASM(2); else QK_ASM(3);
  return xo;
}
#define PV_ASM(I)                                                                                                   \
  asm volatile("v_cvt_f32_i32 %4, %8\n\t"                                                                           \
               "v_cvt_f32_i32 %5, %9\n\t"                                                                           \
               "v_cvt_f32_i32 %6, %10\n\t"                                                                          \
               "v_cvt_f32_i32 %7, %11\n\t"                                                                          \
               "v_mul_f32_dpp %0, %12, %0 quad_perm:[" #I "," #I "," #I "," #I "] row_mask:0xf bank_mask:0xf bound_ctrl:1\n\t" \
               "v_mul_f32_dpp %1, %12, %1 quad_perm:[" #I "," #I "," #I "," #I "] row_mask:0xf bank_mask:0xf bound_ctrl:1\n\t" \
               "v_mul_f32_dpp %2, %12, %2 quad_perm:[" #I "," #I "," #I "," #I "] row_mask:0xf bank_mask:0xf bound_ctrl:1\n\t" \
               "v_mul_f32_dpp %3, %12, %3 quad_perm:[" #I "," #I "," #I "," #I "] row_mask:0xf bank_mask:0xf bound_ctrl:1\n\t" \
               "v_fmac_f32_dpp %0, %13, %4 quad_perm:[" #I "," #I "," #I "," #I "] row_mask:0xf bank_mask:0xf bound_ctrl:1\n\t" \
               "v_fmac_f32_dpp %1, %13, %5 quad_perm:[" #I "," #I "," #I "," #I "] row_mask:0xf bank_mask:0xf bound_ctrl:1\n\t" \
               "v_fmac_f32_dpp %2, %13, %6 quad_perm:[" #I "," #I "," #I "," #I "] row_mask:0xf bank_mask:0xf bound_ctrl:1\n\t" \
               "v_fmac_f32_dpp %3, %13, %7 quad_perm:[" #I "," #I "," #I "," #I "] row_mask:0xf bank_mask:0xf bound_ctrl:1"     \
               : "+v"(a0), "+v"(a1), "+v"(a2), "+v"(a3), "=&v"(t0), "=&v"(t1), "=&v"(t2), "=&v"(t3)                 \
               : "v"(v0), "v"(v1), "v"(v2), "v"(v3), "v"(alpha), "v"(pv)                                            \
               : "memory")
template <int I>
__device__ __forceinline__ void pv_head(float& a0, float& a1, float& a2, float& a3, const u32x4& v, float alpha, float pv) {
  float t0, t1, t2, t3;
  const unsigned v0 = v[0], v1 = v[1], v2 = v[2], v3 = v[3];
  if constexpr (I == 0) PV_ASM(0); else if constexpr (I == 1) PV_ASM(1); else if constexpr (I == 2) PV_ASM(2); else PV_ASM(3);
}

struct T_ { static constexpr bool value = true; };
struct F_ { static constexpr bool value = false; };

struct Args {
  const unsigned short* q;
  const int* qk;
  const unsigned short* ksc;
  const unsigned short* kzp;
  const int* qv;
  const unsigned short* vsc;
  const unsigned short* vzp;
  const unsigned short* qcos;
  const unsigned short* qsin;
  const unsigned short* kcos;
  const unsigned short* ksin;
  unsigned char* ws;
  __hip_bfloat16* out;
};

struct Unit {
  u32x4 k[HG];
  u32x4 v[HG];
  u32x2 c;
  u32x2 s;
};

constexpr int REC = 132;
constexpr int LDS_SC = 0;
constexpr int LDS_QAB = TOK_WG * HG * 4;
constexpr int LDS_MRG = LDS_QAB + HG * D;
constexpr int LDS_USED = LDS_MRG + WAVES * 2 * HG * REC;
constexpr int LDS_FLOATS = LDSK * 1024;
static_assert(LDS_USED <= LDS_FLOATS, "");

__global__ void __launch_bounds__(NTHR) attn_decode(Args a) {
  __shared__ __attribute__((aligned(16))) float lds[LDS_FLOATS];
  f32x4* sc4 = reinterpret_cast<f32x4*>(lds + LDS_SC);
  unsigned* qabl = reinterpret_cast<unsigned*>(lds + LDS_QAB);
  float* mrg = lds + LDS_MRG;
  const unsigned long long t_start = __builtin_amdgcn_s_memrealtime();
  const int split = blockIdx.x % NSPLIT;
  const int grp = blockIdx.x / NSPLIT;
  const int tid = threadIdx.x;
  const int lane = tid & 63;
  const int wave = __builtin_amdgcn_readfirstlane(tid >> 6);
  const int r = lane >> 5, c = lane & 31;
  const int cls = c & 3;
  const int bh0 = grp * HG;
  const int tw0 = split * TOK_WG;

  const auto krs = __builtin_amdgcn_make_buffer_rsrc((void*)(reinterpret_cast<const char*>(a.qk) + (size_t)bh0 * HSTRIDE), 0, HG * HSTRIDE, 0x00020000);
  const auto vrs = __builtin_amdgcn_make_buffer_rsrc((void*)(reinterpret_cast<const char*>(a.qv) + (size_t)bh0 * HSTRIDE), 0, HG * HSTRIDE, 0x00020000);
  const auto crs = __builtin_amdgcn_make_buffer_rsrc((void*)a.kcos, 0, SKV * D * 2, 0x00020000);
  const auto srs = __builtin_amdgcn_make_buffer_rsrc((void*)a.ksin, 0, SKV * D * 2, 0x00020000);
  const auto wrs = __builtin_amdgcn_make_buffer_rsrc((void*)a.ws, 0, WS_END, 0x00020000);
  const unsigned koff = (unsigned)(tw0 + 2 * wave) * (D * 4) + lane * 16;
  const unsigned coff = (unsigned)(tw0 + 2 * wave) * (D * 2) + lane * 8;
  const unsigned soff = (unsigned)(tw0 + 2 * wave) * (D * 2) + ((lane * 8) ^ 128);
  constexpr unsigned KSTEP = WAVES * 1024, CSTEP = WAVES * 512;

  auto ld_t = [&](Unit& U, unsigned u) {
    const unsigned o = u < (unsigned)NUNIT ? u * CSTEP : 0x40000000u;
    U.c = __builtin_amdgcn_raw_buffer_load_b64(crs, coff + o, 0, TABAUX);
    U.s = __builtin_amdgcn_raw_buffer_load_b64(srs, soff + o, 0, TABAUX);
  };
  auto ld_k = [&](Unit& U, unsigned u, int i) { U.k[i] = __builtin_amdgcn_raw_buffer_load_b128(krs, koff + (u < (unsigned)NUNIT ? u * KSTEP : 0x80000000u), i * HSTRIDE, KAUX); };
  auto ld_v = [&](Unit& U, unsigned u, int i) { U.v[i] = __builtin_amdgcn_raw_buffer_load_b128(vrs, koff + (u < (unsigned)NUNIT ? u * KSTEP : 0x80000000u), i * HSTRIDE, VAUX); };
  auto issue = [&](Unit& U, unsigned u) {
    ld_t(U, u);
#pragma unroll
    for (int i = 0; i < HG; ++i) ld_k(U, u, i);
#pragma unroll
    for (int i = 0; i < HG; ++i) ld_v(U, u, i);
  };

  Unit UA, UB;
  const int qh = wave & (HG - 1);
  const unsigned short* qp = a.q + (bh0 + qh) * D;
  const unsigned short q_lo = qp[lane], q_hi = qp[lane + 64];
  const unsigned short c_lo = a.qcos[lane], c_hi = a.qcos[lane + 64];
  const unsigned short s_lo = a.qsin[lane], s_hi = a.qsin[lane + 64];
  constexpr int NITEM = HG * TOK_WG / 2 / NTHR;
  unsigned sraw[NITEM][4];
#pragma unroll
  for (int j = 0; j < NITEM; ++j) {
    const int it = tid + NTHR * j;
    const int hh = it / (TOK_WG / 2), pp = it % (TOK_WG / 2);
    const size_t sidx = (size_t)(bh0 + hh) * SKV + tw0 + 2 * pp;
    sraw[j][0] = LDSC(reinterpret_cast<const unsigned*>(a.ksc + sidx));
    sraw[j][1] = LDSC(reinterpret_cast<const unsigned*>(a.kzp + sidx));
    sraw[j][2] = LDSC(reinterpret_cast<const unsigned*>(a.vsc + sidx));
    sraw[j][3] = LDSC(reinterpret_cast<const unsigned*>(a.vzp + sidx));
  }
  const unsigned tag = __builtin_amdgcn_raw_buffer_load_b32(wrs, WS_EPOCH + grp * 256, 0, 16) + 1u;
  __builtin_amdgcn_sched_barrier(0);

  issue(UA, 0);
  __builtin_amdgcn_sched_barrier(0);
  issue(UB, 1);
  __builtin_amdgcn_sched_barrier(0);

  {
    const float q1 = bfu(q_lo), q2 = bfu(q_hi);
    const float r1 = rbf(rbf(q1 * bfu(c_lo)) + rbf(-q2 * bfu(s_lo)));
    const float r2 = rbf(rbf(q2 * bfu(c_hi)) + rbf(q1 * bfu(s_hi)));
    if (wave < HG) {
      qabl[qh * D + lane] = packbf(r1, r2);
      qabl[qh * D + lane + 64] = packbf(r2, -r1);
    }
  }
#pragma unroll
  for (int j = 0; j < NITEM; ++j) {
    const int it = tid + NTHR * j;
    const int hh = it / (TOK_WG / 2), pp = it % (TOK_WG / 2);
    f32x4 a0 = {__uint_as_float(sraw[j][0] << 16) * SCALE_LOG2E, __uint_as_float(sraw[j][1] << 16), __uint_as_float(sraw[j][2] << 16), __uint_as_float(sraw[j][3] << 16)};
    f32x4 a1 = {__uint_as_float(sraw[j][0] & 0xffff0000u) * SCALE_LOG2E, __uint_as_float(sraw[j][1] & 0xffff0000u), __uint_as_float(sraw[j][2] & 0xffff0000u), __uint_as_float(sraw[j][3] & 0xffff0000u)};
    sc4[(2 * pp) * HG + hh] = a0;
    sc4[(2 * pp + 1) * HG + hh] = a1;
  }
  __syncthreads();
  u32x4 qab[HG];
#pragma unroll
  for (int i = 0; i < HG; ++i) qab[i] = *reinterpret_cast<const u32x4*>(qabl + i * D + 4 * c);

  float m = -INFINITY, l = 0.f, corr = 0.f;
  float acc[HG][4];
#pragma unroll
  for (int i = 0; i < HG; ++i)
#pragma unroll
    for (int j = 0; j < 4; ++j) acc[i][j] = 0.f;
  const bool b0 = c & 1, b1 = c & 2;
  const f32x4* scp = sc4 + (2 * wave + r) * HG + cls;

  auto step = [&](Unit& U, unsigned u, auto next_tag) {
    constexpr bool NEXT = decltype(next_tag)::value;
    const f32x4 sc = scp[u * (2 * WAVES * HG)];
    const unsigned cs0 = __builtin_amdgcn_perm(U.s[0], U.c[0], 0x05040100u);
    const unsigned cs1 = __builtin_amdgcn_perm(U.s[0], U.c[0], 0x07060302u);
    const unsigned cs2 = __builtin_amdgcn_perm(U.s[1], U.c[1], 0x05040100u);
    const unsigned cs3 = __builtin_amdgcn_perm(U.s[1], U.c[1], 0x07060302u);
    float x[HG];
    x[0] = qk_head<0>(U.k[0], cs0, cs1, cs2, cs3, qab[0], sc[1]);
    if constexpr (NEXT && ROLL) { ld_t(U, u + 2); ld_k(U, u + 2, 0); __builtin_amdgcn_sched_barrier(0); }
    x[1] = qk_head<1>(U.k[1], cs0, cs1, cs2, cs3, qab[1], sc[1]);
    if constexpr (NEXT && ROLL) { ld_k(U, u + 2, 1); __builtin_amdgcn_sched_barrier(0); }
    x[2] = qk_head<2>(U.k[2], cs0, cs1, cs2, cs3, qab[2], sc[1]);
    if constexpr (NEXT && ROLL) { ld_k(U, u + 2, 2); __builtin_amdgcn_sched_barrier(0); }
    x[3] = qk_head<3>(U.k[3], cs0, cs1, cs2, cs3, qab[3], sc[1]);
    if constexpr (NEXT && ROLL) { ld_k(U, u + 2, 3); __builtin_amdgcn_sched_barrier(0); }
    const float e0 = x[0] + dpp<XOR1>(x[0]), e1 = x[1] + dpp<XOR1>(x[1]);
    const float e2 = x[2] + dpp<XOR1>(x[2]), e3 = x[3] + dpp<XOR1>(x[3]);
    const float y0 = b0 ? e1 : e0;
    const float y1 = b0 ? e3 : e2;
    const float g0 = y0 + dpp<XOR2>(y0), g1 = y1 + dpp<XOR2>(y1);
    float z = b1 ? g1 : g0;
    z += dpp<ROR4>(z);
    z += dpp<ROR8>(z);
    z = x16_sum(z);
    const float s = z * sc[0];
    const float mn = fmaxf(m, s);
    const float alpha = __builtin_amdgcn_exp2f(m - mn);
    const float p = __builtin_amdgcn_exp2f(s - mn);
    m = mn;
    l = fmaf(l, alpha, p);
    const float pv = p * sc[2];
    corr = fmaf(corr, alpha, pv * sc[3]);
    pv_head<0>(acc[0][0], acc[0][1], acc[0][2], acc[0][3], U.v[0], alpha, pv);
    if constexpr (NEXT && ROLL) { ld_v(U, u + 2, 0); __builtin_amdgcn_sched_barrier(0); }
    pv_head<1>(acc[1][0], acc[1][1], acc[1][2], acc[1][3], U.v[1], alpha, pv);
    if constexpr (NEXT && ROLL) { ld_v(U, u + 2, 1); __builtin_amdgcn_sched_barrier(0); }
    pv_head<2>(acc[2][0], acc[2][1], acc[2][2], acc[2][3], U.v[2], alpha, pv);
    if constexpr (NEXT && ROLL) { ld_v(U, u + 2, 2); __builtin_amdgcn_sched_barrier(0); }
    pv_head<3>(acc[3][0], acc[3][1], acc[3][2], acc[3][3], U.v[3], alpha, pv);
    if constexpr (NEXT && ROLL) { ld_v(U, u + 2, 3); __builtin_amdgcn_sched_barrier(0); }
    if constexpr (NEXT && !ROLL) { issue(U, u + 2); __builtin_amdgcn_sched_barrier(0); }
  };

#pragma unroll 1
  for (unsigned u = 0; u < NUNIT - 2; u += 2) {
    step(UA, u, T_{});
    step(UB, u + 1, T_{});
  }
  step(UA, NUNIT - 2, F_{});
  step(UB, NUNIT - 1, F_{});

  {
    float* rec = mrg + (wave * 2 + r) * HG * REC;
#pragma unroll
    for (int i = 0; i < HG; ++i) *reinterpret_cast<f32x4*>(rec + i * REC + 4 + 4 * c) = f32x4{acc[i][0], acc[i][1], acc[i][2], acc[i][3]};
    if (c < HG) *reinterpret_cast<f32x4*>(rec + c * REC) = f32x4{m, l, corr, 0.f};
  }
  __syncthreads();
  if (wave >= HG) return;
  {
    const int hh = wave;
    constexpr int NR = WAVES * 2;
    f32x4 ml[NR]; f32x2 av[NR];
#pragma unroll
    for (int w = 0; w < NR; ++w) {
      const float* rec = mrg + (w * HG + hh) * REC;
      ml[w] = *reinterpret_cast<const f32x4*>(rec);
      av[w] = *reinterpret_cast<const f32x2*>(rec + 4 + 2 * lane);
    }
    float M = ml[0][0];
#pragma unroll
    for (int w = 1; w < NR; ++w) M = fmaxf(M, ml[w][0]);
    float L = 0.f, C = 0.f, o0 = 0.f, o1 = 0.f;
#pragma unroll
    for (int w = 0; w < NR; ++w) {
      const float f = __builtin_amdgcn_exp2f(ml[w][0] - M);
      L = fmaf(f, ml[w][1], L);
      C = fmaf(f, ml[w][2], C);
      o0 = fmaf(f, av[w][0], o0);
      o1 = fmaf(f, av[w][1], o1);
    }
    o0 -= C; o1 -= C;
    const unsigned aoff = WS_ACC + ((((unsigned)(grp * HG + hh) * 4 + (lane >> 4)) * NSPLIT + split) * 256) + (lane & 15) * 16;
    __builtin_amdgcn_raw_buffer_store_b128(u32x4{__float_as_uint(o0), tag, __float_as_uint(o1), tag}, wrs, aoff, 0, 16);
    if (lane == 0) {
      const unsigned moff = WS_ML + ((unsigned)(grp * HG + hh) * NSPLIT + split) * 16;
      __builtin_amdgcn_raw_buffer_store_b128(u32x4{__float_as_uint(M), tag, __float_as_uint(L), tag}, wrs, moff, 0, 16);
    }
  }
  if (wave != 0) return;
  {
    const int hh = split >> 2, sl = split & 3;
    const unsigned abase = WS_ACC + (((unsigned)(grp * HG + hh) * 4 + sl) * NSPLIT) * 256 + lane * 16;
    const unsigned mbase = WS_ML + ((unsigned)(grp * HG + hh) * NSPLIT + (lane >> 4)) * 16;
    u32x4 av[4], mv[4];
    bool ok = false;
    for (unsigned spins = 0; spins < (1u << 18); ++spins) {
      asm volatile("" ::: "memory");
#pragma unroll
      for (int n = 0; n < 4; ++n) {
        av[n] = __builtin_amdgcn_raw_buffer_load_b128(wrs, abase + n * 1024, 0, 16);
        mv[n] = __builtin_amdgcn_raw_buffer_load_b128(wrs, mbase + n * 64, 0, 16);
      }
      unsigned bad = 0;
#pragma unroll
      for (int n = 0; n < 4; ++n) bad |= (av[n][1] ^ tag) | (av[n][3] ^ tag) | (mv[n][1] ^ tag) | (mv[n][3] ^ tag);
      if (__all(bad == 0)) { ok = true; break; }
      __builtin_amdgcn_s_sleep(PSLEEP);
    }
    float M = -INFINITY;
#pragma unroll
    for (int n = 0; n < 4; ++n) M = fmaxf(M, __uint_as_float(mv[n][0]));
    M = x32_max(x16_max(M));
    float L = 0.f, o0 = 0.f, o1 = 0.f;
#pragma unroll
    for (int n = 0; n < 4; ++n) {
      const float f = __builtin_amdgcn_exp2f(__uint_as_float(mv[n][0]) - M);
      L = fmaf(f, __uint_as_float(mv[n][2]), L);
      o0 = fmaf(f, __uint_as_float(av[n][0]), o0);
      o1 = fmaf(f, __uint_as_float(av[n][2]), o1);
    }
    L = x32_sum(x16_sum(L));
    o0 = x32_sum(x16_sum(o0));
    o1 = x32_sum(x16_sum(o1));
    const float inv = ok ? 1.f / L : __uint_as_float(0x7fc00000u);
    if (lane < 16) {
      *reinterpret_cast<unsigned*>(a.out + (bh0 + hh) * D + sl * 32 + 2 * lane) = packbf(o0 * inv, o1 * inv);
    }
    if (split == 0 && wave == 0 && lane == 0 && ok) __builtin_amdgcn_raw_buffer_store_b32(tag, wrs, WS_EPOCH + grp * 256, 0, 16);
  }
  while (__builtin_amdgcn_s_memrealtime() - t_start < FIXSPAN) __builtin_amdgcn_s_sleep(1);
}

}

extern "C" void kernel_launch(void* const* d_in, const int* in_sizes, int n_in,
                              void* d_out, int out_size, void* d_ws, size_t ws_size,
                              hipStream_t stream) {
  Args a;
  a.q = (const unsigned short*)d_in[0];
  a.qk = (const int*)d_in[1];
  a.ksc = (const unsigned short*)d_in[2];
  a.kzp = (const unsigned short*)d_in[3];
  a.qv = (const int*)d_in[4];
  a.vsc = (const unsigned short*)d_in[5];
  a.vzp = (const unsigned short*)d_in[6];
  a.qcos = (const unsigned short*)d_in[7];
  a.qsin = (const unsigned short*)d_in[8];
  a.kcos = (const unsigned short*)d_in[9];
  a.ksin = (const unsigned short*)d_in[10];
  a.ws = (unsigned char*)d_ws;
  a.out = (__hip_bfloat16*)d_out;
  attn_decode<<<NWG, NTHR, 0, stream>>>(a);
}
```

```cpp
#include <hip/hip_runtime.h>
#include <hip/hip_bf16.h>
#include <stdint.h>

#define WAVES_ 4
#define TABAUX 0
#define KAUX 2
#define VAUX 2
#define SCNT 0
#define LDSC(p) (*(p))
#define ROLL 1
#define FIRST 0
#define POLLW 1
#define PSTAG 4
#define SKIPSEEN 0
#define EARLYWB 0
#define OUTSC1 0
#define LDSK 21
#define NB 2
#define PEEL 1
#define PSLEEP 1

namespace {

constexpr int B_ = 2, H_ = 32, SKV = 4096, D = 128;
constexpr int BH = B_ * H_;
constexpr int HG = 4;
constexpr int NGRP = BH / HG;
constexpr int NWG = 256;
constexpr int NSPLIT = NWG / NGRP;
constexpr int WAVES = WAVES_;
constexpr int NTHR = WAVES * 64;
constexpr int TOK_WG = SKV / NSPLIT;
constexpr int NUNIT = TOK_WG / 2 / WAVES;
constexpr float SCALE_LOG2E = 0.088388347648318440f * 1.4426950408889634f;
constexpr unsigned HSTRIDE = (unsigned)SKV * D * 4;

constexpr unsigned WS_EPOCH = 0;
constexpr unsigned WS_ML = 4096;
constexpr unsigned WS_ACC = WS_ML + NGRP * HG * NSPLIT * 16;
constexpr unsigned WS_END = WS_ACC + NGRP * HG * 4 * NSPLIT * 256;

using u32x4 = __attribute__((ext_vector_type(4))) unsigned;
using u32x2 = __attribute__((ext_vector_type(2))) unsigned;
using f32x4 = __attribute__((ext_vector_type(4))) float;
using f32x2 = __attribute__((ext_vector_type(2))) float;
using bf16x2 = __attribute__((ext_vector_type(2))) __bf16;

__device__ __forceinline__ float rbf(float x) { return (float)(__bf16)x; }
__device__ __forceinline__ float bfu(unsigned short h) { return __uint_as_float((unsigned)h << 16); }
__device__ __forceinline__ unsigned packbf(float lo, float hi) {
  bf16x2 v; v[0] = (__bf16)lo; v[1] = (__bf16)hi;
  return __builtin_bit_cast(unsigned, v);
}
template <int CTRL> __device__ __forceinline__ float dpp(float x) {
  return __builtin_bit_cast(float, __builtin_amdgcn_mov_dpp(__builtin_bit_cast(int, x), CTRL, 0xf, 0xf, true));
}
constexpr int XOR1 = 0xB1, XOR2 = 0x4E, ROR4 = 0x124, ROR8 = 0x128;
__device__ __forceinline__ float x16_sum(float x) {
  auto s = __builtin_amdgcn_permlane16_swap(__float_as_uint(x), __float_as_uint(x), false, false);
  return __uint_as_float(s[0]) + __uint_as_float(s[1]);
}
__device__ __forceinline__ float x16_max(float x) {
  auto s = __builtin_amdgcn_permlane16_swap(__float_as_uint(x), __float_as_uint(x), false, false);
  return fmaxf(__uint_as_float(s[0]), __uint_as_float(s[1]));
}
__device__ __forceinline__ float x32_sum(float x) {
  auto s = __builtin_amdgcn_permlane32_swap(__float_as_uint(x), __float_as_uint(x), false, false);
  return __uint_as_float(s[0]) + __uint_as_float(s[1]);
}
__device__ __forceinline__ float x32_max(float x) {
  auto s = __builtin_amdgcn_permlane32_swap(__float_as_uint(x), __float_as_uint(x), false, false);
  return fmaxf(__uint_as_float(s[0]), __uint_as_float(s[1]));
}

#define QK_ASM(I)                                                                                                   \
  asm volatile("v_dot2_f32_bf16 %1, %13, %17, 0\n\t"                                                                \
               "v_dot2_f32_bf16 %2, %14, %18, 0\n\t"                                                                \
               "v_dot2_f32_bf16 %3, %15, %19, 0\n\t"                                                                \
               "v_dot2_f32_bf16 %4, %16, %20, 0\n\t"                                                                \
               "v_cvt_f32_i32 %5, %9\n\t"                                                                           \
               "v_cvt_f32_i32 %6, %10\n\t"                                                                          \
               "v_cvt_f32_i32 %7, %11\n\t"                                                                          \
               "v_cvt_f32_i32 %8, %12\n\t"                                                                          \
               "v_subrev_f32_dpp %5, %21, %5 quad_perm:[" #I "," #I "," #I "," #I "] row_mask:0xf bank_mask:0xf bound_ctrl:1\n\t" \
               "v_subrev_f32_dpp %6, %21, %6 quad_perm:[" #I "," #I "," #I "," #I "] row_mask:0xf bank_mask:0xf bound_ctrl:1\n\t" \
               "v_subrev_f32_dpp %7, %21, %7 quad_perm:[" #I "," #I "," #I "," #I "] row_mask:0xf bank_mask:0xf bound_ctrl:1\n\t" \
               "v_subrev_f32_dpp %8, %21, %8 quad_perm:[" #I "," #I "," #I "," #I "] row_mask:0xf bank_mask:0xf bound_ctrl:1\n\t" \
               "v_mul_f32 %0, %5, %1\n\t"                                                                           \
               "v_fmac_f32 %0, %6, %2\n\t"                                                                          \
               "v_fmac_f32 %0, %7, %3\n\t"                                                                          \
               "v_fmac_f32 %0, %8, %4"                                                                              \
               : "=&v"(xo), "=&v"(w0), "=&v"(w1), "=&v"(w2), "=&v"(w3), "=&v"(t0), "=&v"(t1), "=&v"(t2), "=&v"(t3)  \
               : "v"(k0), "v"(k1), "v"(k2), "v"(k3), "v"(cs0), "v"(cs1), "v"(cs2), "v"(cs3), "v"(q0), "v"(q1), "v"(q2), "v"(q3), "v"(kz) \
               : "memory")
template <int I>
__device__ __forceinline__ float qk_head(const u32x4& k, unsigned cs0, unsigned cs1, unsigned cs2, unsigned cs3, const u32x4& q, float kz) {
  float xo, w0, w1, w2, w3, t0, t1, t2, t3;
  const unsigned k0 = k[0], k1 = k[1], k2 = k[2], k3 = k[3], q0 = q[0], q1 = q[1], q2 = q[2], q3 = q[3];
  if constexpr (I == 0) QK_ASM(0); else if constexpr (I == 1) QK_ASM(1); else if constexpr (I == 2) QK_ASM(2); else QK_ASM(3);
  return xo;
}
#define PV_ASM(I)                                                                                                   \
  asm volatile("v_cvt_f32_i32 %4, %8\n\t"                                                                           \
               "v_cvt_f32_i32 %5, %9\n\t"                                                                           \
               "v_cvt_f32_i32 %6, %10\n\t"                                                                          \
               "v_cvt_f32_i32 %7, %11\n\t"                                                                          \
               "v_mul_f32_dpp %0, %12, %0 quad_perm:[" #I "," #I "," #I "," #I "] row_mask:0xf bank_mask:0xf bound_ctrl:1\n\t" \
               "v_mul_f32_dpp %1, %12, %1 quad_perm:[" #I "," #I "," #I "," #I "] row_mask:0xf bank_mask:0xf bound_ctrl:1\n\t" \
               "v_mul_f32_dpp %2, %12, %2 quad_perm:[" #I "," #I "," #I "," #I "] row_mask:0xf bank_mask:0xf bound_ctrl:1\n\t" \
               "v_mul_f32_dpp %3, %12, %3 quad_perm:[" #I "," #I "," #I "," #I "] row_mask:0xf bank_mask:0xf bound_ctrl:1\n\t" \
               "v_fmac_f32_dpp %0, %13, %4 quad_perm:[" #I "," #I "," #I "," #I "] row_mask:0xf bank_mask:0xf bound_ctrl:1\n\t" \
               "v_fmac_f32_dpp %1, %13, %5 quad_perm:[" #I "," #I "," #I "," #I "] row_mask:0xf bank_mask:0xf bound_ctrl:1\n\t" \
               "v_fmac_f32_dpp %2, %13, %6 quad_perm:[" #I "," #I "," #I "," #I "] row_mask:0xf bank_mask:0xf bound_ctrl:1\n\t" \
               "v_fmac_f32_dpp %3, %13, %7 quad_perm:[" #I "," #I "," #I "," #I "] row_mask:0xf bank_mask:0xf bound_ctrl:1"     \
               : "+v"(a0), "+v"(a1), "+v"(a2), "+v"(a3), "=&v"(t0), "=&v"(t1), "=&v"(t2), "=&v"(t3)                 \
               : "v"(v0), "v"(v1), "v"(v2), "v"(v3), "v"(alpha), "v"(pv)                                            \
               : "memory")
template <int I>
__device__ __forceinline__ void pv_head(float& a0, float& a1, float& a2, float& a3, const u32x4& v, float alpha, float pv) {
  float t0, t1, t2, t3;
  const unsigned v0 = v[0], v1 = v[1], v2 = v[2], v3 = v[3];
  if constexpr (I == 0) PV_ASM(0); else if constexpr (I == 1) PV_ASM(1); else if constexpr (I == 2) PV_ASM(2); else PV_ASM(3);
}

struct T_ { static constexpr bool value = true; };
struct F_ { static constexpr bool value = false; };

struct Args {
  const unsigned short* q;
  const int* qk;
  const unsigned short* ksc;
  const unsigned short* kzp;
  const int* qv;
  const unsigned short* vsc;
  const unsigned short* vzp;
  const unsigned short* qcos;
  const unsigned short* qsin;
  const unsigned short* kcos;
  const unsigned short* ksin;
  unsigned char* ws;
  __hip_bfloat16* out;
};

struct Unit {
  u32x4 k[HG];
  u32x4 v[HG];
  u32x2 c;
  u32x2 s;
};

constexpr int REC = 132;
constexpr int LDS_SC = 0;
constexpr int LDS_QAB = TOK_WG * HG * 4;
constexpr int LDS_MRG = LDS_QAB + HG * D;
constexpr int LDS_USED = LDS_MRG + WAVES * 2 * HG * REC;
constexpr int LDS_FLOATS = LDSK * 1024;
static_assert(LDS_USED <= LDS_FLOATS, "");

__global__ void __launch_bounds__(NTHR) attn_decode(Args a) {
  __shared__ __attribute__((aligned(16))) float lds[LDS_FLOATS];
  f32x4* sc4 = reinterpret_cast<f32x4*>(lds + LDS_SC);
  unsigned* qabl = reinterpret_cast<unsigned*>(lds + LDS_QAB);
  float* mrg = lds + LDS_MRG;
  const int split = blockIdx.x % NSPLIT;
  const int grp = blockIdx.x / NSPLIT;
  const int tid = threadIdx.x;
  const int lane = tid & 63;
  const int wave = __builtin_amdgcn_readfirstlane(tid >> 6);
  const int r = lane >> 5, c = lane & 31;
  const int cls = c & 3;
  const int bh0 = grp * HG;
  const int tw0 = split * TOK_WG;

  const auto krs = __builtin_amdgcn_make_buffer_rsrc((void*)(reinterpret_cast<const char*>(a.qk) + (size_t)bh0 * HSTRIDE), 0, HG * HSTRIDE, 0x00020000);
  const auto vrs = __builtin_amdgcn_make_buffer_rsrc((void*)(reinterpret_cast<const char*>(a.qv) + (size_t)bh0 * HSTRIDE), 0, HG * HSTRIDE, 0x00020000);
  const auto crs = __builtin_amdgcn_make_buffer_rsrc((void*)a.kcos, 0, SKV * D * 2, 0x00020000);
  const auto srs = __builtin_amdgcn_make_buffer_rsrc((void*)a.ksin, 0, SKV * D * 2, 0x00020000);
  const auto wrs = __builtin_amdgcn_make_buffer_rsrc((void*)a.ws, 0, WS_END, 0x00020000);
  const unsigned koff = (unsigned)(tw0 + 2 * wave) * (D * 4) + lane * 16;
  const unsigned coff = (unsigned)(tw0 + 2 * wave) * (D * 2) + lane * 8;
  const unsigned soff = (unsigned)(tw0 + 2 * wave) * (D * 2) + ((lane * 8) ^ 128);
  constexpr unsigned KSTEP = WAVES * 1024, CSTEP = WAVES * 512;

  auto ld_t = [&](Unit& U, unsigned u) {
    const unsigned o = u < (unsigned)NUNIT ? u * CSTEP : 0x40000000u;
    U.c = __builtin_amdgcn_raw_buffer_load_b64(crs, coff + o, 0, TABAUX);
    U.s = __builtin_amdgcn_raw_buffer_load_b64(srs, soff + o, 0, TABAUX);
  };
  auto ld_k = [&](Unit& U, unsigned u, int i) { U.k[i] = __builtin_amdgcn_raw_buffer_load_b128(krs, koff + (u < (unsigned)NUNIT ? u * KSTEP : 0x80000000u), i * HSTRIDE, KAUX); };
  auto ld_v = [&](Unit& U, unsigned u, int i) { U.v[i] = __builtin_amdgcn_raw_buffer_load_b128(vrs, koff + (u < (unsigned)NUNIT ? u * KSTEP : 0x80000000u), i * HSTRIDE, VAUX); };
  auto issue = [&](Unit& U, unsigned u) {
    ld_t(U, u);
#pragma unroll
    for (int i = 0; i < HG; ++i) ld_k(U, u, i);
#pragma unroll
    for (int i = 0; i < HG; ++i) ld_v(U, u, i);
  };

  Unit UA, UB;
  const int qh = wave & (HG - 1);
  const unsigned short* qp = a.q + (bh0 + qh) * D;
  const unsigned short q_lo = qp[lane], q_hi = qp[lane + 64];
  const unsigned short c_lo = a.qcos[lane], c_hi = a.qcos[lane + 64];
  const unsigned short s_lo = a.qsin[lane], s_hi = a.qsin[lane + 64];
  constexpr int NITEM = HG * TOK_WG / 2 / NTHR;
  unsigned sraw[NITEM][4];
#pragma unroll
  for (int j = 0; j < NITEM; ++j) {
    const int it = tid + NTHR * j;
    const int hh = it / (TOK_WG / 2), pp = it % (TOK_WG / 2);
    const size_t sidx = (size_t)(bh0 + hh) * SKV + tw0 + 2 * pp;
    sraw[j][0] = LDSC(reinterpret_cast<const unsigned*>(a.ksc + sidx));
    sraw[j][1] = LDSC(reinterpret_cast<const unsigned*>(a.kzp + sidx));
    sraw[j][2] = LDSC(reinterpret_cast<const unsigned*>(a.vsc + sidx));
    sraw[j][3] = LDSC(reinterpret_cast<const unsigned*>(a.vzp + sidx));
  }
  const unsigned tag = __builtin_amdgcn_raw_buffer_load_b32(wrs, WS_EPOCH + grp * 256, 0, 16) + 1u;
  __builtin_amdgcn_sched_barrier(0);

  issue(UA, 0);
  __builtin_amdgcn_sched_barrier(0);
  issue(UB, 1);
  __builtin_amdgcn_sched_barrier(0);

  {
    const float q1 = bfu(q_lo), q2 = bfu(q_hi);
    const float r1 = rbf(rbf(q1 * bfu(c_lo)) + rbf(-q2 * bfu(s_lo)));
    const float r2 = rbf(rbf(q2 * bfu(c_hi)) + rbf(q1 * bfu(s_hi)));
    if (wave < HG) {
      qabl[qh * D + lane] = packbf(r1, r2);
      qabl[qh * D + lane + 64] = packbf(r2, -r1);
    }
  }
#pragma unroll
  for (int j = 0; j < NITEM; ++j) {
    const int it = tid + NTHR * j;
    const int hh = it / (TOK_WG / 2), pp = it % (TOK_WG / 2);
    f32x4 a0 = {__uint_as_float(sraw[j][0] << 16) * SCALE_LOG2E, __uint_as_float(sraw[j][1] << 16), __uint_as_float(sraw[j][2] << 16), __uint_as_float(sraw[j][3] << 16)};
    f32x4 a1 = {__uint_as_float(sraw[j][0] & 0xffff0000u) * SCALE_LOG2E, __uint_as_float(sraw[j][1] & 0xffff0000u), __uint_as_float(sraw[j][2] & 0xffff0000u), __uint_as_float(sraw[j][3] & 0xffff0000u)};
    sc4[(2 * pp) * HG + hh] = a0;
    sc4[(2 * pp + 1) * HG + hh] = a1;
  }
  __syncthreads();
  u32x4 qab[HG];
#pragma unroll
  for (int i = 0; i < HG; ++i) qab[i] = *reinterpret_cast<const u32x4*>(qabl + i * D + 4 * c);

  float m = -INFINITY, l = 0.f, corr = 0.f;
  float acc[HG][4];
#pragma unroll
  for (int i = 0; i < HG; ++i)
#pragma unroll
    for (int j = 0; j < 4; ++j) acc[i][j] = 0.f;
  const bool b0 = c & 1, b1 = c & 2;
  const f32x4* scp = sc4 + (2 * wave + r) * HG + cls;

  auto step = [&](Unit& U, unsigned u, auto next_tag) {
    constexpr bool NEXT = decltype(next_tag)::value;
    const f32x4 sc = scp[u * (2 * WAVES * HG)];
    const unsigned cs0 = __builtin_amdgcn_perm(U.s[0], U.c[0], 0x05040100u);
    const unsigned cs1 = __builtin_amdgcn_perm(U.s[0], U.c[0], 0x07060302u);
    const unsigned cs2 = __builtin_amdgcn_perm(U.s[1], U.c[1], 0x05040100u);
    const unsigned cs3 = __builtin_amdgcn_perm(U.s[1], U.c[1], 0x07060302u);
    float x[HG];
    x[0] = qk_head<0>(U.k[0], cs0, cs1, cs2, cs3, qab[0], sc[1]);
    if constexpr (NEXT && ROLL) { ld_t(U, u + NB); ld_k(U, u + NB, 0); __builtin_amdgcn_sched_barrier(0); }
    x[1] = qk_head<1>(U.k[1], cs0, cs1, cs2, cs3, qab[1], sc[1]);
    if constexpr (NEXT && ROLL) { ld_k(U, u + NB, 1); __builtin_amdgcn_sched_barrier(0); }
    x[2] = qk_head<2>(U.k[2], cs0, cs1, cs2, cs3, qab[2], sc[1]);
    if constexpr (NEXT && ROLL) { ld_k(U, u + NB, 2); __builtin_amdgcn_sched_barrier(0); }
    x[3] = qk_head<3>(U.k[3], cs0, cs1, cs2, cs3, qab[3], sc[1]);
    if constexpr (NEXT && ROLL) { ld_k(U, u + NB, 3); __builtin_amdgcn_sched_barrier(0); }
    const float e0 = x[0] + dpp<XOR1>(x[0]), e1 = x[1] + dpp<XOR1>(x[1]);
    const float e2 = x[2] + dpp<XOR1>(x[2]), e3 = x[3] + dpp<XOR1>(x[3]);
    const float y0 = b0 ? e1 : e0;
    const float y1 = b0 ? e3 : e2;
    const float g0 = y0 + dpp<XOR2>(y0), g1 = y1 + dpp<XOR2>(y1);
    float z = b1 ? g1 : g0;
    z += dpp<ROR4>(z);
    z += dpp<ROR8>(z);
    z = x16_sum(z);
    const float s = z * sc[0];
    const float mn = fmaxf(m, s);
    const float alpha = __builtin_amdgcn_exp2f(m - mn);
    const float p = __builtin_amdgcn_exp2f(s - mn);
    m = mn;
    l = fmaf(l, alpha, p);
    const float pv = p * sc[2];
    corr = fmaf(corr, alpha, pv * sc[3]);
    pv_head<0>(acc[0][0], acc[0][1], acc[0][2], acc[0][3], U.v[0], alpha, pv);
    if constexpr (NEXT && ROLL) { ld_v(U, u + NB, 0); __builtin_amdgcn_sched_barrier(0); }
    pv_head<1>(acc[1][0], acc[1][1], acc[1][2], acc[1][3], U.v[1], alpha, pv);
    if constexpr (NEXT && ROLL) { ld_v(U, u + NB, 1); __builtin_amdgcn_sched_barrier(0); }
    pv_head<2>(acc[2][0], acc[2][1], acc[2][2], acc[2][3], U.v[2], alpha, pv);
    if constexpr (NEXT && ROLL) { ld_v(U, u + NB, 2); __builtin_amdgcn_sched_barrier(0); }
    pv_head<3>(acc[3][0], acc[3][1], acc[3][2], acc[3][3], U.v[3], alpha, pv);
    if constexpr (NEXT && ROLL) { ld_v(U, u + NB, 3); __builtin_amdgcn_sched_barrier(0); }
    if constexpr (NEXT && !ROLL) { issue(U, u + NB); __builtin_amdgcn_sched_barrier(0); }
  };

#pragma unroll 1
  for (unsigned u = 0; u < NUNIT - 2; u += 2) {
    step(UA, u, T_{});
    step(UB, u + 1, T_{});
  }
  step(UA, NUNIT - 2, F_{});
  step(UB, NUNIT - 1, F_{});

  {
    float* rec = mrg + (wave * 2 + r) * HG * REC;
#pragma unroll
    for (int i = 0; i < HG; ++i) *reinterpret_cast<f32x4*>(rec + i * REC + 4 + 4 * c) = f32x4{acc[i][0], acc[i][1], acc[i][2], acc[i][3]};
    if (c < HG) *reinterpret_cast<f32x4*>(rec + c * REC) = f32x4{m, l, corr, 0.f};
  }
  __syncthreads();
  if (wave >= HG) return;
  {
    const int hh = wave;
    constexpr int NR = WAVES * 2;
    f32x4 ml[NR]; f32x2 av[NR];
#pragma unroll
    for (int w = 0; w < NR; ++w) {
      const float* rec = mrg + (w * HG + hh) * REC;
      ml[w] = *reinterpret_cast<const f32x4*>(rec);
      av[w] = *reinterpret_cast<const f32x2*>(rec + 4 + 2 * lane);
    }
    float M = ml[0][0];
#pragma unroll
    for (int w = 1; w < NR; ++w) M = fmaxf(M, ml[w][0]);
    float L = 0.f, C = 0.f, o0 = 0.f, o1 = 0.f;
#pragma unroll
    for (int w = 0; w < NR; ++w) {
      const float f = __builtin_amdgcn_exp2f(ml[w][0] - M);
      L = fmaf(f, ml[w][1], L);
      C = fmaf(f, ml[w][2], C);
      o0 = fmaf(f, av[w][0], o0);
      o1 = fmaf(f, av[w][1], o1);
    }
    o0 -= C; o1 -= C;
    const unsigned aoff = WS_ACC + ((((unsigned)(grp * HG + hh) * 4 + (lane >> 4)) * NSPLIT + split) * 256) + (lane & 15) * 16;
    __builtin_amdgcn_raw_buffer_store_b128(u32x4{__float_as_uint(o0), tag, __float_as_uint(o1), tag}, wrs, aoff, 0, 16);
    if (lane == 0) {
      const unsigned moff = WS_ML + ((unsigned)(grp * HG + hh) * NSPLIT + split) * 16;
      __builtin_amdgcn_raw_buffer_store_b128(u32x4{__float_as_uint(M), tag, __float_as_uint(L), tag}, wrs, moff, 0, 16);
    }
  }
  if (wave != 0) return;
  {
    const int hh = split >> 2, sl = split & 3;
    const unsigned abase = WS_ACC + (((unsigned)(grp * HG + hh) * 4 + sl) * NSPLIT) * 256 + lane * 16;
    const unsigned mbase = WS_ML + ((unsigned)(grp * HG + hh) * NSPLIT + (lane >> 4)) * 16;
    u32x4 av[4], mv[4];
    bool ok = false;
    for (unsigned spins = 0; spins < (1u << 18); ++spins) {
      asm volatile("" ::: "memory");
#pragma unroll
      for (int n = 0; n < 4; ++n) {
        av[n] = __builtin_amdgcn_raw_buffer_load_b128(wrs, abase + n * 1024, 0, 16);
        mv[n] = __builtin_amdgcn_raw_buffer_load_b128(wrs, mbase + n * 64, 0, 16);
      }
      unsigned bad = 0;
#pragma unroll
      for (int n = 0; n < 4; ++n) bad |= (av[n][1] ^ tag) | (av[n][3] ^ tag) | (mv[n][1] ^ tag) | (mv[n][3] ^ tag);
      if (__all(bad == 0)) { ok = true; break; }
      __builtin_amdgcn_s_sleep(PSLEEP);
    }
    float M = -INFINITY;
#pragma unroll
    for (int n = 0; n < 4; ++n) M = fmaxf(M, __uint_as_float(mv[n][0]));
    M = x32_max(x16_max(M));
    float L = 0.f, o0 = 0.f, o1 = 0.f;
#pragma unroll
    for (int n = 0; n < 4; ++n) {
      const float f = __builtin_amdgcn_exp2f(__uint_as_float(mv[n][0]) - M);
      L = fmaf(f, __uint_as_float(mv[n][2]), L);
      o0 = fmaf(f, __uint_as_float(av[n][0]), o0);
      o1 = fmaf(f, __uint_as_float(av[n][2]), o1);
    }
    L = x32_sum(x16_sum(L));
    o0 = x32_sum(x16_sum(o0));
    o1 = x32_sum(x16_sum(o1));
    const float inv = ok ? 1.f / L : __uint_as_float(0x7fc00000u);
    if (lane < 16) {
      *reinterpret_cast<unsigned*>(a.out + (bh0 + hh) * D + sl * 32 + 2 * lane) = packbf(o0 * inv, o1 * inv);
    }
    if (split == 0 && wave == 0 && lane == 0 && ok) __builtin_amdgcn_raw_buffer_store_b32(tag, wrs, WS_EPOCH + grp * 256, 0, 16);
  }
}

}

extern "C" void kernel_launch(void* const* d_in, const int* in_sizes, int n_in,
                              void* d_out, int out_size, void* d_ws, size_t ws_size,
                              hipStream_t stream) {
  Args a;
  a.q = (const unsigned short*)d_in[0];
  a.qk = (const int*)d_in[1];
  a.ksc = (const unsigned short*)d_in[2];
  a.kzp = (const unsigned short*)d_in[3];
  a.qv = (const int*)d_in[4];
  a.vsc = (const unsigned short*)d_in[5];
  a.vzp = (const unsigned short*)d_in[6];
  a.qcos = (const unsigned short*)d_in[7];
  a.qsin = (const unsigned short*)d_in[8];
  a.kcos = (const unsigned short*)d_in[9];
  a.ksin = (const unsigned short*)d_in[10];
  a.ws = (unsigned char*)d_ws;
  a.out = (__hip_bfloat16*)d_out;
  attn_decode<<<NWG, NTHR, 0, stream>>>(a);
}
```

```cpp
#include <hip/hip_runtime.h>
#include <hip/hip_bf16.h>
#include <stdint.h>

namespace {

constexpr int B_ = 2, H_ = 32, SKV = 4096, D = 128;
constexpr int BH = B_ * H_;
constexpr int HG = 4;
constexpr int NGRP = BH / HG;
constexpr int NWG = 256;
constexpr int NSPLIT = NWG / NGRP;
constexpr int WAVES = 4;
constexpr int NTHR = WAVES * 64;
constexpr int TOK_WG = SKV / NSPLIT;
constexpr int NUNIT = TOK_WG / 2 / WAVES;
constexpr float SCALE_LOG2E = 0.088388347648318440f * 1.4426950408889634f;
constexpr unsigned HSTRIDE = (unsigned)SKV * D * 4;
constexpr int AUX_NT = 2, AUX_SC1 = 16;

constexpr unsigned WS_EPOCH = 0;
constexpr unsigned WS_ML = 4096;
constexpr unsigned WS_ACC = WS_ML + NGRP * HG * NSPLIT * 16;
constexpr unsigned WS_END = WS_ACC + NGRP * HG * 4 * NSPLIT * 256;

using u32x4 = __attribute__((ext_vector_type(4))) unsigned;
using u32x2 = __attribute__((ext_vector_type(2))) unsigned;
using f32x4 = __attribute__((ext_vector_type(4))) float;
using f32x2 = __attribute__((ext_vector_type(2))) float;
using bf16x2 = __attribute__((ext_vector_type(2))) __bf16;

__device__ __forceinline__ float rbf(float x) { return (float)(__bf16)x; }
__device__ __forceinline__ float bfu(unsigned short h) { return __uint_as_float((unsigned)h << 16); }
__device__ __forceinline__ unsigned packbf(float lo, float hi) {
  bf16x2 v; v[0] = (__bf16)lo; v[1] = (__bf16)hi;
  return __builtin_bit_cast(unsigned, v);
}
template <int CTRL> __device__ __forceinline__ float dpp(float x) {
  return __builtin_bit_cast(float, __builtin_amdgcn_mov_dpp(__builtin_bit_cast(int, x), CTRL, 0xf, 0xf, true));
}
constexpr int XOR1 = 0xB1, XOR2 = 0x4E, ROR4 = 0x124, ROR8 = 0x128;
__device__ __forceinline__ float x16_sum(float x) {
  auto s = __builtin_amdgcn_permlane16_swap(__float_as_uint(x), __float_as_uint(x), false, false);
  return __uint_as_float(s[0]) + __uint_as_float(s[1]);
}
__device__ __forceinline__ float x16_max(float x) {
  auto s = __builtin_amdgcn_permlane16_swap(__float_as_uint(x), __float_as_uint(x), false, false);
  return fmaxf(__uint_as_float(s[0]), __uint_as_float(s[1]));
}
__device__ __forceinline__ float x32_sum(float x) {
  auto s = __builtin_amdgcn_permlane32_swap(__float_as_uint(x), __float_as_uint(x), false, false);
  return __uint_as_float(s[0]) + __uint_as_float(s[1]);
}
__device__ __forceinline__ float x32_max(float x) {
  auto s = __builtin_amdgcn_permlane32_swap(__float_as_uint(x), __float_as_uint(x), false, false);
  return fmaxf(__uint_as_float(s[0]), __uint_as_float(s[1]));
}

#define PERM_(I) "quad_perm:[" #I "," #I "," #I "," #I "] row_mask:0xf bank_mask:0xf bound_ctrl:1"
#define QK_ASM(I)                                                                                                   \
  asm volatile("v_dot2_f32_bf16 %1, %13, %17, 0\n\t"                                                                \
               "v_dot2_f32_bf16 %2, %14, %18, 0\n\t"                                                                \
               "v_dot2_f32_bf16 %3, %15, %19, 0\n\t"                                                                \
               "v_dot2_f32_bf16 %4, %16, %20, 0\n\t"                                                                \
               "v_cvt_f32_i32 %5, %9\n\t"                                                                           \
               "v_cvt_f32_i32 %6, %10\n\t"                                                                          \
               "v_cvt_f32_i32 %7, %11\n\t"                                                                          \
               "v_cvt_f32_i32 %8, %12\n\t"                                                                          \
               "v_subrev_f32_dpp %5, %21, %5 " PERM_(I) "\n\t"                                                      \
               "v_subrev_f32_dpp %6, %21, %6 " PERM_(I) "\n\t"                                                      \
               "v_subrev_f32_dpp %7, %21, %7 " PERM_(I) "\n\t"                                                      \
               "v_subrev_f32_dpp %8, %21, %8 " PERM_(I) "\n\t"                                                      \
               "v_mul_f32 %0, %5, %1\n\t"                                                                           \
               "v_fmac_f32 %0, %6, %2\n\t"                                                                          \
               "v_fmac_f32 %0, %7, %3\n\t"                                                                          \
               "v_fmac_f32 %0, %8, %4"                                                                              \
               : "=&v"(xo), "=&v"(w0), "=&v"(w1), "=&v"(w2), "=&v"(w3), "=&v"(t0), "=&v"(t1), "=&v"(t2), "=&v"(t3)  \
               : "v"(k0), "v"(k1), "v"(k2), "v"(k3), "v"(cs0), "v"(cs1), "v"(cs2), "v"(cs3), "v"(q0), "v"(q1), "v"(q2), "v"(q3), "v"(kz) \
               : "memory")
template <int I>
__device__ __forceinline__ float qk_head(const u32x4& k, unsigned cs0, unsigned cs1, unsigned cs2, unsigned cs3, const u32x4& q, float kz) {
  float xo, w0, w1, w2, w3, t0, t1, t2, t3;
  const unsigned k0 = k[0], k1 = k[1], k2 = k[2], k3 = k[3], q0 = q[0], q1 = q[1], q2 = q[2], q3 = q[3];
  if constexpr (I == 0) QK_ASM(0); else if constexpr (I == 1) QK_ASM(1); else if constexpr (I == 2) QK_ASM(2); else QK_ASM(3);
  return xo;
}
#define PV_ASM(I)                                                                                                   \
  asm volatile("v_cvt_f32_i32 %4, %8\n\t"                                                                           \
               "v_cvt_f32_i32 %5, %9\n\t"                                                                           \
               "v_cvt_f32_i32 %6, %10\n\t"                                                                          \
               "v_cvt_f32_i32 %7, %11\n\t"                                                                          \
               "v_mul_f32_dpp %0, %12, %0 " PERM_(I) "\n\t"                                                         \
               "v_mul_f32_dpp %1, %12, %1 " PERM_(I) "\n\t"                                                         \
               "v_mul_f32_dpp %2, %12, %2 " PERM_(I) "\n\t"                                                         \
               "v_mul_f32_dpp %3, %12, %3 " PERM_(I) "\n\t"                                                         \
               "v_fmac_f32_dpp %0, %13, %4 " PERM_(I) "\n\t"                                                        \
               "v_fmac_f32_dpp %1, %13, %5 " PERM_(I) "\n\t"                                                        \
               "v_fmac_f32_dpp %2, %13, %6 " PERM_(I) "\n\t"                                                        \
               "v_fmac_f32_dpp %3, %13, %7 " PERM_(I)                                                               \
               : "+v"(a0), "+v"(a1), "+v"(a2), "+v"(a3), "=&v"(t0), "=&v"(t1), "=&v"(t2), "=&v"(t3)                 \
               : "v"(v0), "v"(v1), "v"(v2), "v"(v3), "v"(alpha), "v"(pv)                                            \
               : "memory")
template <int I>
__device__ __forceinline__ void pv_head(float (&a)[4], const u32x4& v, float alpha, float pv) {
  float t0, t1, t2, t3;
  float &a0 = a[0], &a1 = a[1], &a2 = a[2], &a3 = a[3];
  const unsigned v0 = v[0], v1 = v[1], v2 = v[2], v3 = v[3];
  if constexpr (I == 0) PV_ASM(0); else if constexpr (I == 1) PV_ASM(1); else if constexpr (I == 2) PV_ASM(2); else PV_ASM(3);
}

struct Yes { static constexpr bool value = true; };
struct No { static constexpr bool value = false; };

struct Args {
  const unsigned short* q;
  const int* qk;
  const unsigned short* ksc;
  const unsigned short* kzp;
  const int* qv;
  const unsigned short* vsc;
  const unsigned short* vzp;
  const unsigned short* qcos;
  const unsigned short* qsin;
  const unsigned short* kcos;
  const unsigned short* ksin;
  unsigned char* ws;
  __hip_bfloat16* out;
};

struct Unit {
  u32x4 k[HG];
  u32x4 v[HG];
  u32x2 c;
  u32x2 s;
};

constexpr int REC = 132;
constexpr int LDS_SC = 0;
constexpr int LDS_QAB = TOK_WG * HG * 4;
constexpr int LDS_MRG = LDS_QAB + HG * D;
constexpr int LDS_USED = LDS_MRG + WAVES * 2 * HG * REC;
constexpr int LDS_FLOATS = 21 * 1024;
static_assert(LDS_USED <= LDS_FLOATS, "LDS map");

__global__ void __launch_bounds__(NTHR) attn_decode(Args a) {
  __shared__ __attribute__((aligned(16))) float lds[LDS_FLOATS];
  f32x4* sc4 = reinterpret_cast<f32x4*>(lds + LDS_SC);
  unsigned* qabl = reinterpret_cast<unsigned*>(lds + LDS_QAB);
  float* mrg = lds + LDS_MRG;

  const int split = blockIdx.x % NSPLIT;
  const int grp = blockIdx.x / NSPLIT;
  const int tid = threadIdx.x;
  const int lane = tid & 63;
  const int wave = __builtin_amdgcn_readfirstlane(tid >> 6);
  const int r = lane >> 5, c = lane & 31;
  const int cls = c & 3;
  const int bh0 = grp * HG;
  const int tw0 = split * TOK_WG;

  const auto krs = __builtin_amdgcn_make_buffer_rsrc((void*)(reinterpret_cast<const char*>(a.qk) + (size_t)bh0 * HSTRIDE), 0, HG * HSTRIDE, 0x00020000);
  const auto vrs = __builtin_amdgcn_make_buffer_rsrc((void*)(reinterpret_cast<const char*>(a.qv) + (size_t)bh0 * HSTRIDE), 0, HG * HSTRIDE, 0x00020000);
  const auto crs = __builtin_amdgcn_make_buffer_rsrc((void*)a.kcos, 0, SKV * D * 2, 0x00020000);
  const auto srs = __builtin_amdgcn_make_buffer_rsrc((void*)a.ksin, 0, SKV * D * 2, 0x00020000);
  const auto wrs = __builtin_amdgcn_make_buffer_rsrc((void*)a.ws, 0, WS_END, 0x00020000);
  const unsigned koff = (unsigned)(tw0 + 2 * wave) * (D * 4) + lane * 16;
  const unsigned coff = (unsigned)(tw0 + 2 * wave) * (D * 2) + lane * 8;
  const unsigned soff = (unsigned)(tw0 + 2 * wave) * (D * 2) + ((lane * 8) ^ 128);
  constexpr unsigned KSTEP = WAVES * 1024, CSTEP = WAVES * 512;

  auto ld_t = [&](Unit& U, unsigned u) {
    U.c = __builtin_amdgcn_raw_buffer_load_b64(crs, coff + u * CSTEP, 0, AUX_NT);
    U.s = __builtin_amdgcn_raw_buffer_load_b64(srs, soff + u * CSTEP, 0, AUX_NT);
  };
  auto ld_k = [&](Unit& U, unsigned u, int i) { U.k[i] = __builtin_amdgcn_raw_buffer_load_b128(krs, koff + u * KSTEP, i * HSTRIDE, AUX_NT); };
  auto ld_v = [&](Unit& U, unsigned u, int i) { U.v[i] = __builtin_amdgcn_raw_buffer_load_b128(vrs, koff + u * KSTEP, i * HSTRIDE, AUX_NT); };
  auto issue = [&](Unit& U, unsigned u) {
    ld_t(U, u);
#pragma unroll
    for (int i = 0; i < HG; ++i) ld_k(U, u, i);
#pragma unroll
    for (int i = 0; i < HG; ++i) ld_v(U, u, i);
  };

  const unsigned short* qp = a.q + (bh0 + wave) * D;
  const unsigned short q_lo = qp[lane], q_hi = qp[lane + 64];
  const unsigned short c_lo = a.qcos[lane], c_hi = a.qcos[lane + 64];
  const unsigned short s_lo = a.qsin[lane], s_hi = a.qsin[lane + 64];
  constexpr int NITEM = HG * TOK_WG / 2 / NTHR;
  unsigned sraw[NITEM][4];
#pragma unroll
  for (int j = 0; j < NITEM; ++j) {
    const int it = tid + NTHR * j;
    const int hh = it / (TOK_WG / 2), pp = it % (TOK_WG / 2);
    const size_t sidx = (size_t)(bh0 + hh) * SKV + tw0 + 2 * pp;
    sraw[j][0] = __builtin_nontemporal_load(reinterpret_cast<const unsigned*>(a.ksc + sidx));
    sraw[j][1] = __builtin_nontemporal_load(reinterpret_cast<const unsigned*>(a.kzp + sidx));
    sraw[j][2] = __builtin_nontemporal_load(reinterpret_cast<const unsigned*>(a.vsc + sidx));
    sraw[j][3] = __builtin_nontemporal_load(reinterpret_cast<const unsigned*>(a.vzp + sidx));
  }
  const unsigned tag = __builtin_amdgcn_raw_buffer_load_b32(wrs, WS_EPOCH + grp * 256, 0, AUX_SC1) + 1u;
  __builtin_amdgcn_sched_barrier(0);

  Unit UA, UB;
  issue(UA, 0);
  __builtin_amdgcn_sched_barrier(0);
  issue(UB, 1);
  __builtin_amdgcn_sched_barrier(0);

  {
    const float q1 = bfu(q_lo), q2 = bfu(q_hi);
    const float r1 = rbf(rbf(q1 * bfu(c_lo)) + rbf(-q2 * bfu(s_lo)));
    const float r2 = rbf(rbf(q2 * bfu(c_hi)) + rbf(q1 * bfu(s_hi)));
    qabl[wave * D + lane] = packbf(r1, r2);
    qabl[wave * D + lane + 64] = packbf(r2, -r1);
  }
#pragma unroll
  for (int j = 0; j < NITEM; ++j) {
    const int it = tid + NTHR * j;
    const int hh = it / (TOK_WG / 2), pp = it % (TOK_WG / 2);
    f32x4 a0 = {__uint_as_float(sraw[j][0] << 16) * SCALE_LOG2E, __uint_as_float(sraw[j][1] << 16), __uint_as_float(sraw[j][2] << 16), __uint_as_float(sraw[j][3] << 16)};
    f32x4 a1 = {__uint_as_float(sraw[j][0] & 0xffff0000u) * SCALE_LOG2E, __uint_as_float(sraw[j][1] & 0xffff0000u), __uint_as_float(sraw[j][2] & 0xffff0000u), __uint_as_float(sraw[j][3] & 0xffff0000u)};
    sc4[(2 * pp) * HG + hh] = a0;
    sc4[(2 * pp + 1) * HG + hh] = a1;
  }
  __syncthreads();
  u32x4 qab[HG];
#pragma unroll
  for (int i = 0; i < HG; ++i) qab[i] = *reinterpret_cast<const u32x4*>(qabl + i * D + 4 * c);

  float m = -INFINITY, l = 0.f, corr = 0.f;
  float acc[HG][4];
#pragma unroll
  for (int i = 0; i < HG; ++i)
#pragma unroll
    for (int j = 0; j < 4; ++j) acc[i][j] = 0.f;
  const bool b0 = c & 1, b1 = c & 2;
  const f32x4* scp = sc4 + (2 * wave + r) * HG + cls;

  auto step = [&](Unit& U, unsigned u, auto next) {
    constexpr bool NEXT = decltype(next)::value;
    const f32x4 sc = scp[u * (2 * WAVES * HG)];
    const unsigned cs0 = __builtin_amdgcn_perm(U.s[0], U.c[0], 0x05040100u);
    const unsigned cs1 = __builtin_amdgcn_perm(U.s[0], U.c[0], 0x07060302u);
    const unsigned cs2 = __builtin_amdgcn_perm(U.s[1], U.c[1], 0x05040100u);
    const unsigned cs3 = __builtin_amdgcn_perm(U.s[1], U.c[1], 0x07060302u);
    float x[HG];
    x[0] = qk_head<0>(U.k[0], cs0, cs1, cs2, cs3, qab[0], sc[1]);
    if constexpr (NEXT) { ld_t(U, u + 2); ld_k(U, u + 2, 0); __builtin_amdgcn_sched_barrier(0); }
    x[1] = qk_head<1>(U.k[1], cs0, cs1, cs2, cs3, qab[1], sc[1]);
    if constexpr (NEXT) { ld_k(U, u + 2, 1); __builtin_amdgcn_sched_barrier(0); }
    x[2] = qk_head<2>(U.k[2], cs0, cs1, cs2, cs3, qab[2], sc[1]);
    if constexpr (NEXT) { ld_k(U, u + 2, 2); __builtin_amdgcn_sched_barrier(0); }
    x[3] = qk_head<3>(U.k[3], cs0, cs1, cs2, cs3, qab[3], sc[1]);
    if constexpr (NEXT) { ld_k(U, u + 2, 3); __builtin_amdgcn_sched_barrier(0); }
    const float e0 = x[0] + dpp<XOR1>(x[0]), e1 = x[1] + dpp<XOR1>(x[1]);
    const float e2 = x[2] + dpp<XOR1>(x[2]), e3 = x[3] + dpp<XOR1>(x[3]);
    const float y0 = b0 ? e1 : e0;
    const float y1 = b0 ? e3 : e2;
    const float g0 = y0 + dpp<XOR2>(y0), g1 = y1 + dpp<XOR2>(y1);
    float z = b1 ? g1 : g0;
    z += dpp<ROR4>(z);
    z += dpp<ROR8>(z);
    z = x16_sum(z);
    const float s = z * sc[0];
    const float mn = fmaxf(m, s);
    const float alpha = __builtin_amdgcn_exp2f(m - mn);
    const float p = __builtin_amdgcn_exp2f(s - mn);
    m = mn;
    l = fmaf(l, alpha, p);
    const float pv = p * sc[2];
    corr = fmaf(corr, alpha, pv * sc[3]);
    pv_head<0>(acc[0], U.v[0], alpha, pv);
    if constexpr (NEXT) { ld_v(U, u + 2, 0); __builtin_amdgcn_sched_barrier(0); }
    pv_head<1>(acc[1], U.v[1], alpha, pv);
    if constexpr (NEXT) { ld_v(U, u + 2, 1); __builtin_amdgcn_sched_barrier(0); }
    pv_head<2>(acc[2], U.v[2], alpha, pv);
    if constexpr (NEXT) { ld_v(U, u + 2, 2); __builtin_amdgcn_sched_barrier(0); }
    pv_head<3>(acc[3], U.v[3], alpha, pv);
    if constexpr (NEXT) { ld_v(U, u + 2, 3); __builtin_amdgcn_sched_barrier(0); }
  };

#pragma unroll 1
  for (unsigned u = 0; u < NUNIT - 2; u += 2) {
    step(UA, u, Yes{});
    step(UB, u + 1, Yes{});
  }
  step(UA, NUNIT - 2, No{});
  step(UB, NUNIT - 1, No{});

  {
    float* rec = mrg + (wave * 2 + r) * HG * REC;
#pragma unroll
    for (int i = 0; i < HG; ++i) *reinterpret_cast<f32x4*>(rec + i * REC + 4 + 4 * c) = f32x4{acc[i][0], acc[i][1], acc[i][2], acc[i][3]};
    if (c < HG) *reinterpret_cast<f32x4*>(rec + c * REC) = f32x4{m, l, corr, 0.f};
  }
  __syncthreads();
  {
    const int hh = wave;
    constexpr int NR = WAVES * 2;
    f32x4 ml[NR]; f32x2 av[NR];
#pragma unroll
    for (int w = 0; w < NR; ++w) {
      const float* rec = mrg + (w * HG + hh) * REC;
      ml[w] = *reinterpret_cast<const f32x4*>(rec);
      av[w] = *reinterpret_cast<const f32x2*>(rec + 4 + 2 * lane);
    }
    float M = ml[0][0];
#pragma unroll
    for (int w = 1; w < NR; ++w) M = fmaxf(M, ml[w][0]);
    float L = 0.f, C = 0.f, o0 = 0.f, o1 = 0.f;
#pragma unroll
    for (int w = 0; w < NR; ++w) {
      const float f = __builtin_amdgcn_exp2f(ml[w][0] - M);
      L = fmaf(f, ml[w][1], L);
      C = fmaf(f, ml[w][2], C);
      o0 = fmaf(f, av[w][0], o0);
      o1 = fmaf(f, av[w][1], o1);
    }
    o0 -= C; o1 -= C;
    const unsigned aoff = WS_ACC + ((((unsigned)(grp * HG + hh) * 4 + (lane >> 4)) * NSPLIT + split) * 256) + (lane & 15) * 16;
    __builtin_amdgcn_raw_buffer_store_b128(u32x4{__float_as_uint(o0), tag, __float_as_uint(o1), tag}, wrs, aoff, 0, AUX_SC1);
    if (lane == 0) {
      const unsigned moff = WS_ML + ((unsigned)(grp * HG + hh) * NSPLIT + split) * 16;
      __builtin_amdgcn_raw_buffer_store_b128(u32x4{__float_as_uint(M), tag, __float_as_uint(L), tag}, wrs, moff, 0, AUX_SC1);
    }
  }
  if (wave != 0) return;
  {
    const int hh = split >> 2, sl = split & 3;
    const unsigned abase = WS_ACC + (((unsigned)(grp * HG + hh) * 4 + sl) * NSPLIT) * 256 + lane * 16;
    const unsigned mbase = WS_ML + ((unsigned)(grp * HG + hh) * NSPLIT + (lane >> 4)) * 16;
    u32x4 av[4], mv[4];
    bool ok = false;
    for (unsigned spins = 0; spins < (1u << 18); ++spins) {
      asm volatile("" ::: "memory");
#pragma unroll
      for (int n = 0; n < 4; ++n) {
        av[n] = __builtin_amdgcn_raw_buffer_load_b128(wrs, abase + n * 1024, 0, AUX_SC1);
        mv[n] = __builtin_amdgcn_raw_buffer_load_b128(wrs, mbase + n * 64, 0, AUX_SC1);
      }
      unsigned bad = 0;
#pragma unroll
      for (int n = 0; n < 4; ++n) bad |= (av[n][1] ^ tag) | (av[n][3] ^ tag) | (mv[n][1] ^ tag) | (mv[n][3] ^ tag);
      if (__all(bad == 0)) { ok = true; break; }
      __builtin_amdgcn_s_sleep(1);
    }
    float M = -INFINITY;
#pragma unroll
    for (int n = 0; n < 4; ++n) M = fmaxf(M, __uint_as_float(mv[n][0]));
    M = x32_max(x16_max(M));
    float L = 0.f, o0 = 0.f, o1 = 0.f;
#pragma unroll
    for (int n = 0; n < 4; ++n) {
      const float f = __builtin_amdgcn_exp2f(__uint_as_float(mv[n][0]) - M);
      L = fmaf(f, __uint_as_float(mv[n][2]), L);
      o0 = fmaf(f, __uint_as_float(av[n][0]), o0);
      o1 = fmaf(f, __uint_as_float(av[n][2]), o1);
    }
    L = x32_sum(x16_sum(L));
    o0 = x32_sum(x16_sum(o0));
    o1 = x32_sum(x16_sum(o1));
    const float inv = ok ? 1.f / L : __uint_as_float(0x7fc00000u);
    if (lane < 16) *reinterpret_cast<unsigned*>(a.out + (bh0 + hh) * D + sl * 32 + 2 * lane) = packbf(o0 * inv, o1 * inv);
    if (split == 0 && lane == 0 && ok) __builtin_amdgcn_raw_buffer_store_b32(tag, wrs, WS_EPOCH + grp * 256, 0, AUX_SC1);
  }
}

}

extern "C" void kernel_launch(void* const* d_in, const int* in_sizes, int n_in,
                              void* d_out, int out_size, void* d_ws, size_t ws_size,
                              hipStream_t stream) {
  Args a;
  a.q = (const unsigned short*)d_in[0];
  a.qk = (const int*)d_in[1];
  a.ksc = (const unsigned short*)d_in[2];
  a.kzp = (const unsigned short*)d_in[3];
  a.qv = (const int*)d_in[4];
  a.vsc = (const unsigned short*)d_in[5];
  a.vzp = (const unsigned short*)d_in[6];
  a.qcos = (const unsigned short*)d_in[7];
  a.qsin = (const unsigned short*)d_in[8];
  a.kcos = (const unsigned short*)d_in[9];
  a.ksin = (const unsigned short*)d_in[10];
  a.ws = (unsigned char*)d_ws;
  a.out = (__hip_bfloat16*)d_out;
  attn_decode<<<NWG, NTHR, 0, stream>>>(a);
}
```

```cpp
#include <hip/hip_runtime.h>
#include <hip/hip_bf16.h>
#include <stdint.h>

#define WAVES_ 4
#define TABAUX 2
#define KAUX 2
#define VAUX 2
#define SCNT 1
#define LDSC(p) __builtin_nontemporal_load(p)
#define ROLL 1
#define FIRST 0
#define POLLW 1
#define PSTAG 4
#define SKIPSEEN 0
#define EARLYWB 0
#define OUTSC1 0
#define LDSK 21
#define NB 2
#define PEEL 1
#define PSLEEP 1

namespace {

constexpr int B_ = 2, H_ = 32, SKV = 4096, D = 128;
constexpr int BH = B_ * H_;
constexpr int HG = 4;
constexpr int NGRP = BH / HG;
constexpr int NWG = 256;
constexpr int NSPLIT = NWG / NGRP;
constexpr int WAVES = WAVES_;
constexpr int NTHR = WAVES * 64;
constexpr int TOK_WG = SKV / NSPLIT;
constexpr int NUNIT = TOK_WG / 2 / WAVES;
constexpr float SCALE_LOG2E = 0.088388347648318440f * 1.4426950408889634f;
constexpr unsigned HSTRIDE = (unsigned)SKV * D * 4;

constexpr unsigned WS_EPOCH = 0;
constexpr unsigned WS_ML = 4096;
constexpr unsigned WS_ACC = WS_ML + NGRP * HG * NSPLIT * 16;
constexpr unsigned WS_END = WS_ACC + NGRP * HG * 4 * NSPLIT * 256;

using u32x4 = __attribute__((ext_vector_type(4))) unsigned;
using u32x2 = __attribute__((ext_vector_type(2))) unsigned;
using f32x4 = __attribute__((ext_vector_type(4))) float;
using f32x2 = __attribute__((ext_vector_type(2))) float;
using bf16x2 = __attribute__((ext_vector_type(2))) __bf16;

__device__ __forceinline__ float rbf(float x) { return (float)(__bf16)x; }
__device__ __forceinline__ float bfu(unsigned short h) { return __uint_as_float((unsigned)h << 16); }
__device__ __forceinline__ unsigned packbf(float lo, float hi) {
  bf16x2 v; v[0] = (__bf16)lo; v[1] = (__bf16)hi;
  return __builtin_bit_cast(unsigned, v);
}
template <int CTRL> __device__ __forceinline__ float dpp(float x) {
  return __builtin_bit_cast(float, __builtin_amdgcn_mov_dpp(__builtin_bit_cast(int, x), CTRL, 0xf, 0xf, true));
}
constexpr int XOR1 = 0xB1, XOR2 = 0x4E, ROR4 = 0x124, ROR8 = 0x128;
__device__ __forceinline__ float x16_sum(float x) {
  auto s = __builtin_amdgcn_permlane16_swap(__float_as_uint(x), __float_as_uint(x), false, false);
  return __uint_as_float(s[0]) + __uint_as_float(s[1]);
}
__device__ __forceinline__ float x16_max(float x) {
  auto s = __builtin_amdgcn_permlane16_swap(__float_as_uint(x), __float_as_uint(x), false, false);
  return fmaxf(__uint_as_float(s[0]), __uint_as_float(s[1]));
}
__device__ __forceinline__ float x32_sum(float x) {
  auto s = __builtin_amdgcn_permlane32_swap(__float_as_uint(x), __float_as_uint(x), false, false);
  return __uint_as_float(s[0]) + __uint_as_float(s[1]);
}
__device__ __forceinline__ float x32_max(float x) {
  auto s = __builtin_amdgcn_permlane32_swap(__float_as_uint(x), __float_as_uint(x), false, false);
  return fmaxf(__uint_as_float(s[0]), __uint_as_float(s[1]));
}

#define QK_ASM(I)                                                                                                   \
  asm volatile("v_dot2_f32_bf16 %1, %13, %17, 0\n\t"                                                                \
               "v_dot2_f32_bf16 %2, %14, %18, 0\n\t"                                                                \
               "v_dot2_f32_bf16 %3, %15, %19, 0\n\t"                                                                \
               "v_dot2_f32_bf16 %4, %16, %20, 0\n\t"                                                                \
               "v_cvt_f32_i32 %5, %9\n\t"                                                                           \
               "v_cvt_f32_i32 %6, %10\n\t"                                                                          \
               "v_cvt_f32_i32 %7, %11\n\t"                                                                          \
               "v_cvt_f32_i32 %8, %12\n\t"                                                                          \
               "v_subrev_f32_dpp %5, %21, %5 quad_perm:[" #I "," #I "," #I "," #I "] row_mask:0xf bank_mask:0xf bound_ctrl:1\n\t" \
               "v_subrev_f32_dpp %6, %21, %6 quad_perm:[" #I "," #I "," #I "," #I "] row_mask:0xf bank_mask:0xf bound_ctrl:1\n\t" \
               "v_subrev_f32_dpp %7, %21, %7 quad_perm:[" #I "," #I "," #I "," #I "] row_mask:0xf bank_mask:0xf bound_ctrl:1\n\t" \
               "v_subrev_f32_dpp %8, %21, %8 quad_perm:[" #I "," #I "," #I "," #I "] row_mask:0xf bank_mask:0xf bound_ctrl:1\n\t" \
               "v_mul_f32 %0, %5, %1\n\t"                                                                           \
               "v_fmac_f32 %0, %6, %2\n\t"                                                                          \
               "v_fmac_f32 %0, %7, %3\n\t"                                                                          \
               "v_fmac_f32 %0, %8, %4"                                                                              \
               : "=&v"(xo), "=&v"(w0), "=&v"(w1), "=&v"(w2), "=&v"(w3), "=&v"(t0), "=&v"(t1), "=&v"(t2), "=&v"(t3)  \
               : "v"(k0), "v"(k1), "v"(k2), "v"(k3), "v"(cs0), "v"(cs1), "v"(cs2), "v"(cs3), "v"(q0), "v"(q1), "v"(q2), "v"(q3), "v"(kz) \
               : "memory")
template <int I>
__device__ __forceinline__ float qk_head(const u32x4& k, unsigned cs0, unsigned cs1, unsigned cs2, unsigned cs3, const u32x4& q, float kz) {
  float xo, w0, w1, w2, w3, t0, t1, t2, t3;
  const unsigned k0 = k[0], k1 = k[1], k2 = k[2], k3 = k[3], q0 = q[0], q1 = q[1], q2 = q[2], q3 = q[3];
  if constexpr (I == 0) QK_ASM(0); else if constexpr (I == 1) QK_ASM(1); else if constexpr (I == 2) QK_ASM(2); else QK_ASM(3);
  return xo;
}
#define PV_ASM(I)                                                                                                   \
  asm volatile("v_cvt_f32_i32 %4, %8\n\t"                                                                           \
               "v_cvt_f32_i32 %5, %9\n\t"                                                                           \
               "v_cvt_f32_i32 %6, %10\n\t"                                                                          \
               "v_cvt_f32_i32 %7, %11\n\t"                                                                          \
               "v_mul_f32_dpp %0, %12, %0 quad_perm:[" #I "," #I "," #I "," #I "] row_mask:0xf bank_mask:0xf bound_ctrl:1\n\t" \
               "v_mul_f32_dpp %1, %12, %1 quad_perm:[" #I "," #I "," #I "," #I "] row_mask:0xf bank_mask:0xf bound_ctrl:1\n\t" \
               "v_mul_f32_dpp %2, %12, %2 quad_perm:[" #I "," #I "," #I "," #I "] row_mask:0xf bank_mask:0xf bound_ctrl:1\n\t" \
               "v_mul_f32_dpp %3, %12, %3 quad_perm:[" #I "," #I "," #I "," #I "] row_mask:0xf bank_mask:0xf bound_ctrl:1\n\t" \
               "v_fmac_f32_dpp %0, %13, %4 quad_perm:[" #I "," #I "," #I "," #I "] row_mask:0xf bank_mask:0xf bound_ctrl:1\n\t" \
               "v_fmac_f32_dpp %1, %13, %5 quad_perm:[" #I "," #I "," #I "," #I "] row_mask:0xf bank_mask:0xf bound_ctrl:1\n\t" \
               "v_fmac_f32_dpp %2, %13, %6 quad_perm:[" #I "," #I "," #I "," #I "] row_mask:0xf bank_mask:0xf bound_ctrl:1\n\t" \
               "v_fmac_f32_dpp %3, %13, %7 quad_perm:[" #I "," #I "," #I "," #I "] row_mask:0xf bank_mask:0xf bound_ctrl:1"     \
               : "+v"(a0), "+v"(a1), "+v"(a2), "+v"(a3), "=&v"(t0), "=&v"(t1), "=&v"(t2), "=&v"(t3)                 \
               : "v"(v0), "v"(v1), "v"(v2), "v"(v3), "v"(alpha), "v"(pv)                                            \
               : "memory")
template <int I>
__device__ __forceinline__ void pv_head(float& a0, float& a1, float& a2, float& a3, const u32x4& v, float alpha, float pv) {
  float t0, t1, t2, t3;
  const unsigned v0 = v[0], v1 = v[1], v2 = v[2], v3 = v[3];
  if constexpr (I == 0) PV_ASM(0); else if constexpr (I == 1) PV_ASM(1); else if constexpr (I == 2) PV_ASM(2); else PV_ASM(3);
}

struct T_ { static constexpr bool value = true; };
struct F_ { static constexpr bool value = false; };

struct Args {
  const unsigned short* q;
  const int* qk;
  const unsigned short* ksc;
  const unsigned short* kzp;
  const int* qv;
  const unsigned short* vsc;
  const unsigned short* vzp;
  const unsigned short* qcos;
  const unsigned short* qsin;
  const unsigned short* kcos;
  const unsigned short* ksin;
  unsigned char* ws;
  __hip_bfloat16* out;
};

struct Unit {
  u32x4 k[HG];
  u32x4 v[HG];
  u32x2 c;
  u32x2 s;
};

constexpr int REC = 132;
constexpr int LDS_SC = 0;
constexpr int LDS_QAB = TOK_WG * HG * 4;
constexpr int LDS_MRG = LDS_QAB + HG * D;
constexpr int LDS_USED = LDS_MRG + WAVES * 2 * HG * REC;
constexpr int LDS_FLOATS = LDSK * 1024;
static_assert(LDS_USED <= LDS_FLOATS, "");

__global__ void __launch_bounds__(NTHR) attn_decode(Args a) {
  __shared__ __attribute__((aligned(16))) float lds[LDS_FLOATS];
  f32x4* sc4 = reinterpret_cast<f32x4*>(lds + LDS_SC);
  unsigned* qabl = reinterpret_cast<unsigned*>(lds + LDS_QAB);
  float* mrg = lds + LDS_MRG;
  const int split = blockIdx.x % NSPLIT;
  const int grp = blockIdx.x / NSPLIT;
  const int tid = threadIdx.x;
  const int lane = tid & 63;
  const int wave = __builtin_amdgcn_readfirstlane(tid >> 6);
  const int r = lane >> 5, c = lane & 31;
  const int cls = c & 3;
  const int bh0 = grp * HG;
  const int tw0 = split * TOK_WG;

  const auto krs = __builtin_amdgcn_make_buffer_rsrc((void*)(reinterpret_cast<const char*>(a.qk) + (size_t)bh0 * HSTRIDE), 0, HG * HSTRIDE, 0x00020000);
  const auto vrs = __builtin_amdgcn_make_buffer_rsrc((void*)(reinterpret_cast<const char*>(a.qv) + (size_t)bh0 * HSTRIDE), 0, HG * HSTRIDE, 0x00020000);
  const auto crs = __builtin_amdgcn_make_buffer_rsrc((void*)a.kcos, 0, SKV * D * 2, 0x00020000);
  const auto srs = __builtin_amdgcn_make_buffer_rsrc((void*)a.ksin, 0, SKV * D * 2, 0x00020000);
  const auto wrs = __builtin_amdgcn_make_buffer_rsrc((void*)a.ws, 0, WS_END, 0x00020000);
  const unsigned koff = (unsigned)(tw0 + 2 * wave) * (D * 4) + lane * 16;
  const unsigned coff = (unsigned)(tw0 + 2 * wave) * (D * 2) + lane * 8;
  const unsigned soff = (unsigned)(tw0 + 2 * wave) * (D * 2) + ((lane * 8) ^ 128);
  constexpr unsigned KSTEP = WAVES * 1024, CSTEP = WAVES * 512;

  auto ld_t = [&](Unit& U, unsigned u) {
    const unsigned o = u < (unsigned)NUNIT ? u * CSTEP : 0x40000000u;
    U.c = __builtin_amdgcn_raw_buffer_load_b64(crs, coff + o, 0, TABAUX);
    U.s = __builtin_amdgcn_raw_buffer_load_b64(srs, soff + o, 0, TABAUX);
  };
  auto ld_k = [&](Unit& U, unsigned u, int i) { U.k[i] = __builtin_amdgcn_raw_buffer_load_b128(krs, koff + (u < (unsigned)NUNIT ? u * KSTEP : 0x80000000u), i * HSTRIDE, KAUX); };
  auto ld_v = [&](Unit& U, unsigned u, int i) { U.v[i] = __builtin_amdgcn_raw_buffer_load_b128(vrs, koff + (u < (unsigned)NUNIT ? u * KSTEP : 0x80000000u), i * HSTRIDE, VAUX); };
  auto issue = [&](Unit& U, unsigned u) {
    ld_t(U, u);
#pragma unroll
    for (int i = 0; i < HG; ++i) ld_k(U, u, i);
#pragma unroll
    for (int i = 0; i < HG; ++i) ld_v(U, u, i);
  };

  Unit UA, UB;
  const int qh = wave & (HG - 1);
  const unsigned short* qp = a.q + (bh0 + qh) * D;
  const unsigned short q_lo = qp[lane], q_hi = qp[lane + 64];
  const unsigned short c_lo = a.qcos[lane], c_hi = a.qcos[lane + 64];
  const unsigned short s_lo = a.qsin[lane], s_hi = a.qsin[lane + 64];
  constexpr int NITEM = HG * TOK_WG / 2 / NTHR;
  unsigned sraw[NITEM][4];
#pragma unroll
  for (int j = 0; j < NITEM; ++j) {
    const int it = tid + NTHR * j;
    const int hh = it / (TOK_WG / 2), pp = it % (TOK_WG / 2);
    const size_t sidx = (size_t)(bh0 + hh) * SKV + tw0 + 2 * pp;
    sraw[j][0] = LDSC(reinterpret_cast<const unsigned*>(a.ksc + sidx));
    sraw[j][1] = LDSC(reinterpret_cast<const unsigned*>(a.kzp + sidx));
    sraw[j][2] = LDSC(reinterpret_cast<const unsigned*>(a.vsc + sidx));
    sraw[j][3] = LDSC(reinterpret_cast<const unsigned*>(a.vzp + sidx));
  }
  const unsigned tag = __builtin_amdgcn_raw_buffer_load_b32(wrs, WS_EPOCH + grp * 256, 0, 16) + 1u;
  __builtin_amdgcn_sched_barrier(0);

  issue(UA, 0);
  __builtin_amdgcn_sched_barrier(0);
  issue(UB, 1);
  __builtin_amdgcn_sched_barrier(0);

  {
    const float q1 = bfu(q_lo), q2 = bfu(q_hi);
    const float r1 = rbf(rbf(q1 * bfu(c_lo)) + rbf(-q2 * bfu(s_lo)));
    const float r2 = rbf(rbf(q2 * bfu(c_hi)) + rbf(q1 * bfu(s_hi)));
    if (wave < HG) {
      qabl[qh * D + lane] = packbf(r1, r2);
      qabl[qh * D + lane + 64] = packbf(r2, -r1);
    }
  }
#pragma unroll
  for (int j = 0; j < NITEM; ++j) {
    const int it = tid + NTHR * j;
    const int hh = it / (TOK_WG / 2), pp = it % (TOK_WG / 2);
    f32x4 a0 = {__uint_as_float(sraw[j][0] << 16) * SCALE_LOG2E, __uint_as_float(sraw[j][1] << 16), __uint_as_float(sraw[j][2] << 16), __uint_as_float(sraw[j][3] << 16)};
    f32x4 a1 = {__uint_as_float(sraw[j][0] & 0xffff0000u) * SCALE_LOG2E, __uint_as_float(sraw[j][1] & 0xffff0000u), __uint_as_float(sraw[j][2] & 0xffff0000u), __uint_as_float(sraw[j][3] & 0xffff0000u)};
    sc4[(2 * pp) * HG + hh] = a0;
    sc4[(2 * pp + 1) * HG + hh] = a1;
  }
  __syncthreads();
  u32x4 qab[HG];
#pragma unroll
  for (int i = 0; i < HG; ++i) qab[i] = *reinterpret_cast<const u32x4*>(qabl + i * D + 4 * c);

  float m = -INFINITY, l = 0.f, corr = 0.f;
  float acc[HG][4];
#pragma unroll
  for (int i = 0; i < HG; ++i)
#pragma unroll
    for (int j = 0; j < 4; ++j) acc[i][j] = 0.f;
  const bool b0 = c & 1, b1 = c & 2;
  const f32x4* scp = sc4 + (2 * wave + r) * HG + cls;

  auto step = [&](Unit& U, unsigned u, auto next_tag) {
    constexpr bool NEXT = decltype(next_tag)::value;
    const f32x4 sc = scp[u * (2 * WAVES * HG)];
    const unsigned cs0 = __builtin_amdgcn_perm(U.s[0], U.c[0], 0x05040100u);
    const unsigned cs1 = __builtin_amdgcn_perm(U.s[0], U.c[0], 0x07060302u);
    const unsigned cs2 = __builtin_amdgcn_perm(U.s[1], U.c[1], 0x05040100u);
    const unsigned cs3 = __builtin_amdgcn_perm(U.s[1], U.c[1], 0x07060302u);
    float x[HG];
    x[0] = qk_head<0>(U.k[0], cs0, cs1, cs2, cs3, qab[0], sc[1]);
    if constexpr (NEXT && ROLL) { ld_t(U, u + NB); ld_k(U, u + NB, 0); __builtin_amdgcn_sched_barrier(0); }
    x[1] = qk_head<1>(U.k[1], cs0, cs1, cs2, cs3, qab[1], sc[1]);
    if constexpr (NEXT && ROLL) { ld_k(U, u + NB, 1); __builtin_amdgcn_sched_barrier(0); }
    x[2] = qk_head<2>(U.k[2], cs0, cs1, cs2, cs3, qab[2], sc[1]);
    if constexpr (NEXT && ROLL) { ld_k(U, u + NB, 2); __builtin_amdgcn_sched_barrier(0); }
    x[3] = qk_head<3>(U.k[3], cs0, cs1, cs2, cs3, qab[3], sc[1]);
    if constexpr (NEXT && ROLL) { ld_k(U, u + NB, 3); __builtin_amdgcn_sched_barrier(0); }
    const float e0 = x[0] + dpp<XOR1>(x[0]), e1 = x[1] + dpp<XOR1>(x[1]);
    const float e2 = x[2] + dpp<XOR1>(x[2]), e3 = x[3] + dpp<XOR1>(x[3]);
    const float y0 = b0 ? e1 : e0;
    const float y1 = b0 ? e3 : e2;
    const float g0 = y0 + dpp<XOR2>(y0), g1 = y1 + dpp<XOR2>(y1);
    float z = b1 ? g1 : g0;
    z += dpp<ROR4>(z);
    z += dpp<ROR8>(z);
    z = x16_sum(z);
    const float s = z * sc[0];
    const float mn = fmaxf(m, s);
    const float alpha = __builtin_amdgcn_exp2f(m - mn);
    const float p = __builtin_amdgcn_exp2f(s - mn);
    m = mn;
    l = fmaf(l, alpha, p);
    const float pv = p * sc[2];
    corr = fmaf(corr, alpha, pv * sc[3]);
    pv_head<0>(acc[0][0], acc[0][1], acc[0][2], acc[0][3], U.v[0], alpha, pv);
    if constexpr (NEXT && ROLL) { ld_v(U, u + NB, 0); __builtin_amdgcn_sched_barrier(0); }
    pv_head<1>(acc[1][0], acc[1][1], acc[1][2], acc[1][3], U.v[1], alpha, pv);
    if constexpr (NEXT && ROLL) { ld_v(U, u + NB, 1); __builtin_amdgcn_sched_barrier(0); }
    pv_head<2>(acc[2][0], acc[2][1], acc[2][2], acc[2][3], U.v[2], alpha, pv);
    if constexpr (NEXT && ROLL) { ld_v(U, u + NB, 2); __builtin_amdgcn_sched_barrier(0); }
    pv_head<3>(acc[3][0], acc[3][1], acc[3][2], acc[3][3], U.v[3], alpha, pv);
    if constexpr (NEXT && ROLL) { ld_v(U, u + NB, 3); __builtin_amdgcn_sched_barrier(0); }
    if constexpr (NEXT && !ROLL) { issue(U, u + NB); __builtin_amdgcn_sched_barrier(0); }
  };

#pragma unroll 1
  for (unsigned u = 0; u < NUNIT - 2; u += 2) {
    step(UA, u, T_{});
    step(UB, u + 1, T_{});
  }
  step(UA, NUNIT - 2, F_{});
  step(UB, NUNIT - 1, F_{});

  {
    float* rec = mrg + (wave * 2 + r) * HG * REC;
#pragma unroll
    for (int i = 0; i < HG; ++i) *reinterpret_cast<f32x4*>(rec + i * REC + 4 + 4 * c) = f32x4{acc[i][0], acc[i][1], acc[i][2], acc[i][3]};
    if (c < HG) *reinterpret_cast<f32x4*>(rec + c * REC) = f32x4{m, l, corr, 0.f};
  }
  __syncthreads();
  if (wave >= HG) return;
  {
    const int hh = wave;
    constexpr int NR = WAVES * 2;
    f32x4 ml[NR]; f32x2 av[NR];
#pragma unroll
    for (int w = 0; w < NR; ++w) {
      const float* rec = mrg + (w * HG + hh) * REC;
      ml[w] = *reinterpret_cast<const f32x4*>(rec);
      av[w] = *reinterpret_cast<const f32x2*>(rec + 4 + 2 * lane);
    }
    float M = ml[0][0];
#pragma unroll
    for (int w = 1; w < NR; ++w) M = fmaxf(M, ml[w][0]);
    float L = 0.f, C = 0.f, o0 = 0.f, o1 = 0.f;
#pragma unroll
    for (int w = 0; w < NR; ++w) {
      const float f = __builtin_amdgcn_exp2f(ml[w][0] - M);
      L = fmaf(f, ml[w][1], L);
      C = fmaf(f, ml[w][2], C);
      o0 = fmaf(f, av[w][0], o0);
      o1 = fmaf(f, av[w][1], o1);
    }
    o0 -= C; o1 -= C;
    const unsigned aoff = WS_ACC + ((((unsigned)(grp * HG + hh) * 4 + (lane >> 4)) * NSPLIT + split) * 256) + (lane & 15) * 16;
    __builtin_amdgcn_raw_buffer_store_b128(u32x4{__float_as_uint(o0), tag, __float_as_uint(o1), tag}, wrs, aoff, 0, 16);
    if (lane == 0) {
      const unsigned moff = WS_ML + ((unsigned)(grp * HG + hh) * NSPLIT + split) * 16;
      __builtin_amdgcn_raw_buffer_store_b128(u32x4{__float_as_uint(M), tag, __float_as_uint(L), tag}, wrs, moff, 0, 16);
    }
  }
  if (wave != 0) return;
  {
    const int hh = split >> 2, sl = split & 3;
    const unsigned abase = WS_ACC + (((unsigned)(grp * HG + hh) * 4 + sl) * NSPLIT) * 256 + lane * 16;
    const unsigned mbase = WS_ML + ((unsigned)(grp * HG + hh) * NSPLIT + (lane >> 4)) * 16;
    u32x4 av[4], mv[4];
    bool ok = false;
    for (unsigned spins = 0; spins < (1u << 18); ++spins) {
      asm volatile("" ::: "memory");
#pragma unroll
      for (int n = 0; n < 4; ++n) {
        av[n] = __builtin_amdgcn_raw_buffer_load_b128(wrs, abase + n * 1024, 0, 16);
        mv[n] = __builtin_amdgcn_raw_buffer_load_b128(wrs, mbase + n * 64, 0, 16);
      }
      unsigned bad = 0;
#pragma unroll
      for (int n = 0; n < 4; ++n) bad |= (av[n][1] ^ tag) | (av[n][3] ^ tag) | (mv[n][1] ^ tag) | (mv[n][3] ^ tag);
      if (__all(bad == 0)) { ok = true; break; }
      __builtin_amdgcn_s_sleep(PSLEEP);
    }
    float M = -INFINITY;
#pragma unroll
    for (int n = 0; n < 4; ++n) M = fmaxf(M, __uint_as_float(mv[n][0]));
    M = x32_max(x16_max(M));
    float L = 0.f, o0 = 0.f, o1 = 0.f;
#pragma unroll
    for (int n = 0; n < 4; ++n) {
      const float f = __builtin_amdgcn_exp2f(__uint_as_float(mv[n][0]) - M);
      L = fmaf(f, __uint_as_float(mv[n][2]), L);
      o0 = fmaf(f, __uint_as_float(av[n][0]), o0);
      o1 = fmaf(f, __uint_as_float(av[n][2]), o1);
    }
    L = x32_sum(x16_sum(L));
    o0 = x32_sum(x16_sum(o0));
    o1 = x32_sum(x16_sum(o1));
    const float inv = ok ? 1.f / L : __uint_as_float(0x7fc00000u);
    if (lane < 16) {
      *reinterpret_cast<unsigned*>(a.out + (bh0 + hh) * D + sl * 32 + 2 * lane) = packbf(o0 * inv, o1 * inv);
    }
    if (split == 0 && wave == 0 && lane == 0 && ok) __builtin_amdgcn_raw_buffer_store_b32(tag, wrs, WS_EPOCH + grp * 256, 0, 16);
  }
}

}

extern "C" void kernel_launch(void* const* d_in, const int* in_sizes, int n_in,
                              void* d_out, int out_size, void* d_ws, size_t ws_size,
                              hipStream_t stream) {
  Args a;
  a.q = (const unsigned short*)d_in[0];
  a.qk = (const int*)d_in[1];
  a.ksc = (const unsigned short*)d_in[2];
  a.kzp = (const unsigned short*)d_in[3];
  a.qv = (const int*)d_in[4];
  a.vsc = (const unsigned short*)d_in[5];
  a.vzp = (const unsigned short*)d_in[6];
  a.qcos = (const unsigned short*)d_in[7];
  a.qsin = (const unsigned short*)d_in[8];
  a.kcos = (const unsigned short*)d_in[9];
  a.ksin = (const unsigned short*)d_in[10];
  a.ws = (unsigned char*)d_ws;
  a.out = (__hip_bfloat16*)d_out;
  attn_decode<<<NWG, NTHR, 0, stream>>>(a);
}
```

```cpp
#include <hip/hip_runtime.h>
#include <hip/hip_bf16.h>
#include <stdint.h>

namespace {

constexpr int B_ = 2, H_ = 32, SKV = 4096, D = 128;
constexpr int BH = B_ * H_;
constexpr int HG = 4;
constexpr int NGRP = BH / HG;
constexpr int NWG = 256;
constexpr int NSPLIT = NWG / NGRP;
constexpr int WAVES = 4;
constexpr int NTHR = WAVES * 64;
constexpr int TOK_WG = SKV / NSPLIT;
constexpr int NUNIT = TOK_WG / 2 / WAVES;
constexpr float SCALE_LOG2E = 0.088388347648318440f * 1.4426950408889634f;
constexpr unsigned HSTRIDE = (unsigned)SKV * D * 4;
constexpr int AUX_NT = 2, AUX_SC1 = 16;

constexpr unsigned WS_EPOCH = 0;
constexpr unsigned WS_ML = 4096;
constexpr unsigned WS_ACC = WS_ML + NGRP * HG * NSPLIT * 16;
constexpr unsigned WS_END = WS_ACC + NGRP * HG * 4 * NSPLIT * 256;

using u32x4 = __attribute__((ext_vector_type(4))) unsigned;
using u32x2 = __attribute__((ext_vector_type(2))) unsigned;
using f32x4 = __attribute__((ext_vector_type(4))) float;
using f32x2 = __attribute__((ext_vector_type(2))) float;
using bf16x2 = __attribute__((ext_vector_type(2))) __bf16;

__device__ __forceinline__ float rbf(float x) { return (float)(__bf16)x; }
__device__ __forceinline__ float bfu(unsigned short h) { return __uint_as_float((unsigned)h << 16); }
__device__ __forceinline__ unsigned packbf(float lo, float hi) {
  bf16x2 v; v[0] = (__bf16)lo; v[1] = (__bf16)hi;
  return __builtin_bit_cast(unsigned, v);
}
template <int CTRL> __device__ __forceinline__ float dpp(float x) {
  return __builtin_bit_cast(float, __builtin_amdgcn_mov_dpp(__builtin_bit_cast(int, x), CTRL, 0xf, 0xf, true));
}
constexpr int XOR1 = 0xB1, XOR2 = 0x4E, ROR4 = 0x124, ROR8 = 0x128;
__device__ __forceinline__ float x16_sum(float x) {
  auto s = __builtin_amdgcn_permlane16_swap(__float_as_uint(x), __float_as_uint(x), false, false);
  return __uint_as_float(s[0]) + __uint_as_float(s[1]);
}
__device__ __forceinline__ float x16_max(float x) {
  auto s = __builtin_amdgcn_permlane16_swap(__float_as_uint(x), __float_as_uint(x), false, false);
  return fmaxf(__uint_as_float(s[0]), __uint_as_float(s[1]));
}
__device__ __forceinline__ float x32_sum(float x) {
  auto s = __builtin_amdgcn_permlane32_swap(__float_as_uint(x), __float_as_uint(x), false, false);
  return __uint_as_float(s[0]) + __uint_as_float(s[1]);
}
__device__ __forceinline__ float x32_max(float x) {
  auto s = __builtin_amdgcn_permlane32_swap(__float_as_uint(x), __float_as_uint(x), false, false);
  return fmaxf(__uint_as_float(s[0]), __uint_as_float(s[1]));
}

#define PERM_(I) "quad_perm:[" #I "," #I "," #I "," #I "] row_mask:0xf bank_mask:0xf bound_ctrl:1"
#define QK_ASM(I)                                                                                                   \
  asm volatile("v_dot2_f32_bf16 %1, %13, %17, 0\n\t"                                                                \
               "v_dot2_f32_bf16 %2, %14, %18, 0\n\t"                                                                \
               "v_dot2_f32_bf16 %3, %15, %19, 0\n\t"                                                                \
               "v_dot2_f32_bf16 %4, %16, %20, 0\n\t"                                                                \
               "v_cvt_f32_i32 %5, %9\n\t"                                                                           \
               "v_cvt_f32_i32 %6, %10\n\t"                                                                          \
               "v_cvt_f32_i32 %7, %11\n\t"                                                                          \
               "v_cvt_f32_i32 %8, %12\n\t"                                                                          \
               "v_subrev_f32_dpp %5, %21, %5 " PERM_(I) "\n\t"                                                      \
               "v_subrev_f32_dpp %6, %21, %6 " PERM_(I) "\n\t"                                                      \
               "v_subrev_f32_dpp %7, %21, %7 " PERM_(I) "\n\t"                                                      \
               "v_subrev_f32_dpp %8, %21, %8 " PERM_(I) "\n\t"                                                      \
               "v_mul_f32 %0, %5, %1\n\t"                                                                           \
               "v_fmac_f32 %0, %6, %2\n\t"                                                                          \
               "v_fmac_f32 %0, %7, %3\n\t"                                                                          \
               "v_fmac_f32 %0, %8, %4"                                                                              \
               : "=&v"(xo), "=&v"(w0), "=&v"(w1), "=&v"(w2), "=&v"(w3), "=&v"(t0), "=&v"(t1), "=&v"(t2), "=&v"(t3)  \
               : "v"(k0), "v"(k1), "v"(k2), "v"(k3), "v"(cs0), "v"(cs1), "v"(cs2), "v"(cs3), "v"(q0), "v"(q1), "v"(q2), "v"(q3), "v"(kz) \
               : "memory")
template <int I>
__device__ __forceinline__ float qk_head(const u32x4& k, unsigned cs0, unsigned cs1, unsigned cs2, unsigned cs3, const u32x4& q, float kz) {
  float xo, w0, w1, w2, w3, t0, t1, t2, t3;
  const unsigned k0 = k[0], k1 = k[1], k2 = k[2], k3 = k[3], q0 = q[0], q1 = q[1], q2 = q[2], q3 = q[3];
  if constexpr (I == 0) QK_ASM(0); else if constexpr (I == 1) QK_ASM(1); else if constexpr (I == 2) QK_ASM(2); else QK_ASM(3);
  return xo;
}
#define PV_ASM(I)                                                                                                   \
  asm volatile("v_cvt_f32_i32 %4, %8\n\t"                                                                           \
               "v_cvt_f32_i32 %5, %9\n\t"                                                                           \
               "v_cvt_f32_i32 %6, %10\n\t"                                                                          \
               "v_cvt_f32_i32 %7, %11\n\t"                                                                          \
               "v_mul_f32_dpp %0, %12, %0 " PERM_(I) "\n\t"                                                         \
               "v_mul_f32_dpp %1, %12, %1 " PERM_(I) "\n\t"                                                         \
               "v_mul_f32_dpp %2, %12, %2 " PERM_(I) "\n\t"                                                         \
               "v_mul_f32_dpp %3, %12, %3 " PERM_(I) "\n\t"                                                         \
               "v_fmac_f32_dpp %0, %13, %4 " PERM_(I) "\n\t"                                                        \
               "v_fmac_f32_dpp %1, %13, %5 " PERM_(I) "\n\t"                                                        \
               "v_fmac_f32_dpp %2, %13, %6 " PERM_(I) "\n\t"                                                        \
               "v_fmac_f32_dpp %3, %13, %7 " PERM_(I)                                                               \
               : "+v"(a0), "+v"(a1), "+v"(a2), "+v"(a3), "=&v"(t0), "=&v"(t1), "=&v"(t2), "=&v"(t3)                 \
               : "v"(v0), "v"(v1), "v"(v2), "v"(v3), "v"(alpha), "v"(pv)                                            \
               : "memory")
template <int I>
__device__ __forceinline__ void pv_head(float (&a)[4], const u32x4& v, float alpha, float pv) {
  float t0, t1, t2, t3;
  float &a0 = a[0], &a1 = a[1], &a2 = a[2], &a3 = a[3];
  const unsigned v0 = v[0], v1 = v[1], v2 = v[2], v3 = v[3];
  if constexpr (I == 0) PV_ASM(0); else if constexpr (I == 1) PV_ASM(1); else if constexpr (I == 2) PV_ASM(2); else PV_ASM(3);
}

struct Yes { static constexpr bool value = true; };
struct No { static constexpr bool value = false; };

struct Args {
  const unsigned short* q;
  const int* qk;
  const unsigned short* ksc;
  const unsigned short* kzp;
  const int* qv;
  const unsigned short* vsc;
  const unsigned short* vzp;
  const unsigned short* qcos;
  const unsigned short* qsin;
  const unsigned short* kcos;
  const unsigned short* ksin;
  unsigned char* ws;
  __hip_bfloat16* out;
};

struct Unit {
  u32x4 k[HG];
  u32x4 v[HG];
  u32x2 c;
  u32x2 s;
};

constexpr int REC = 132;
constexpr int LDS_SC = 0;
constexpr int LDS_QAB = TOK_WG * HG * 4;
constexpr int LDS_MRG = LDS_QAB + HG * D;
constexpr int LDS_USED = LDS_MRG + WAVES * 2 * HG * REC;
constexpr int LDS_FLOATS = 21 * 1024;
typedef char lds_map_fits[(LDS_USED <= LDS_FLOATS) ? 1 : -1];

__global__ void __launch_bounds__(NTHR) attn_decode(Args a) {
  __shared__ __attribute__((aligned(16))) float lds[LDS_FLOATS];
  f32x4* sc4 = reinterpret_cast<f32x4*>(lds + LDS_SC);
  unsigned* qabl = reinterpret_cast<unsigned*>(lds + LDS_QAB);
  float* mrg = lds + LDS_MRG;

  const int split = blockIdx.x % NSPLIT;
  const int grp = blockIdx.x / NSPLIT;
  const int tid = threadIdx.x;
  const int lane = tid & 63;
  const int wave = __builtin_amdgcn_readfirstlane(tid >> 6);
  const int r = lane >> 5, c = lane & 31;
  const int cls = c & 3;
  const int bh0 = grp * HG;
  const int tw0 = split * TOK_WG;

  const auto krs = __builtin_amdgcn_make_buffer_rsrc((void*)(reinterpret_cast<const char*>(a.qk) + (size_t)bh0 * HSTRIDE), 0, HG * HSTRIDE, 0x00020000);
  const auto vrs = __builtin_amdgcn_make_buffer_rsrc((void*)(reinterpret_cast<const char*>(a.qv) + (size_t)bh0 * HSTRIDE), 0, HG * HSTRIDE, 0x00020000);
  const auto crs = __builtin_amdgcn_make_buffer_rsrc((void*)a.kcos, 0, SKV * D * 2, 0x00020000);
  const auto srs = __builtin_amdgcn_make_buffer_rsrc((void*)a.ksin, 0, SKV * D * 2, 0x00020000);
  const auto wrs = __builtin_amdgcn_make_buffer_rsrc((void*)a.ws, 0, WS_END, 0x00020000);
  const unsigned koff = (unsigned)(tw0 + 2 * wave) * (D * 4) + lane * 16;
  const unsigned coff = (unsigned)(tw0 + 2 * wave) * (D * 2) + lane * 8;
  const unsigned soff = (unsigned)(tw0 + 2 * wave) * (D * 2) + ((lane * 8) ^ 128);
  constexpr unsigned KSTEP = WAVES * 1024, CSTEP = WAVES * 512;

  auto ld_t = [&](Unit& U, unsigned u) {
    U.c = __builtin_amdgcn_raw_buffer_load_b64(crs, coff + u * CSTEP, 0, AUX_NT);
    U.s = __builtin_amdgcn_raw_buffer_load_b64(srs, soff + u * CSTEP, 0, AUX_NT);
  };
  auto ld_k = [&](Unit& U, unsigned u, int i) { U.k[i] = __builtin_amdgcn_raw_buffer_load_b128(krs, koff + u * KSTEP, i * HSTRIDE, AUX_NT); };
  auto ld_v = [&](Unit& U, unsigned u, int i) { U.v[i] = __builtin_amdgcn_raw_buffer_load_b128(vrs, koff + u * KSTEP, i * HSTRIDE, AUX_NT); };
  auto issue = [&](Unit& U, unsigned u) {
    ld_t(U, u);
#pragma unroll
    for (int i = 0; i < HG; ++i) ld_k(U, u, i);
#pragma unroll
    for (int i = 0; i < HG; ++i) ld_v(U, u, i);
  };

  const unsigned short* qp = a.q + (bh0 + wave) * D;
  const unsigned short q_lo = qp[lane], q_hi = qp[lane + 64];
  const unsigned short c_lo = a.qcos[lane], c_hi = a.qcos[lane + 64];
  const unsigned short s_lo = a.qsin[lane], s_hi = a.qsin[lane + 64];
  constexpr int NITEM = HG * TOK_WG / 2 / NTHR;
  unsigned sraw[NITEM][4];
#pragma unroll
  for (int j = 0; j < NITEM; ++j) {
    const int it = tid + NTHR * j;
    const int hh = it / (TOK_WG / 2), pp = it % (TOK_WG / 2);
    const size_t sidx = (size_t)(bh0 + hh) * SKV + tw0 + 2 * pp;
    sraw[j][0] = __builtin_nontemporal_load(reinterpret_cast<const unsigned*>(a.ksc + sidx));
    sraw[j][1] = __builtin_nontemporal_load(reinterpret_cast<const unsigned*>(a.kzp + sidx));
    sraw[j][2] = __builtin_nontemporal_load(reinterpret_cast<const unsigned*>(a.vsc + sidx));
    sraw[j][3] = __builtin_nontemporal_load(reinterpret_cast<const unsigned*>(a.vzp + sidx));
  }
  const unsigned tag = __builtin_amdgcn_raw_buffer_load_b32(wrs, WS_EPOCH + grp * 256, 0, AUX_SC1) + 1u;
  __builtin_amdgcn_sched_barrier(0);

  Unit UA, UB;
  issue(UA, 0);
  __builtin_amdgcn_sched_barrier(0);
  issue(UB, 1);
  __builtin_amdgcn_sched_barrier(0);

  {
    const float q1 = bfu(q_lo), q2 = bfu(q_hi);
    const float r1 = rbf(rbf(q1 * bfu(c_lo)) + rbf(-q2 * bfu(s_lo)));
    const float r2 = rbf(rbf(q2 * bfu(c_hi)) + rbf(q1 * bfu(s_hi)));
    qabl[wave * D + lane] = packbf(r1, r2);
    qabl[wave * D + lane + 64] = packbf(r2, -r1);
  }
#pragma unroll
  for (int j = 0; j < NITEM; ++j) {
    const int it = tid + NTHR * j;
    const int hh = it / (TOK_WG / 2), pp = it % (TOK_WG / 2);
    f32x4 a0 = {__uint_as_float(sraw[j][0] << 16) * SCALE_LOG2E, __uint_as_float(sraw[j][1] << 16), __uint_as_float(sraw[j][2] << 16), __uint_as_float(sraw[j][3] << 16)};
    f32x4 a1 = {__uint_as_float(sraw[j][0] & 0xffff0000u) * SCALE_LOG2E, __uint_as_float(sraw[j][1] & 0xffff0000u), __uint_as_float(sraw[j][2] & 0xffff0000u), __uint_as_float(sraw[j][3] & 0xffff0000u)};
    sc4[(2 * pp) * HG + hh] = a0;
    sc4[(2 * pp + 1) * HG + hh] = a1;
  }
  __syncthreads();
  u32x4 qab[HG];
#pragma unroll
  for (int i = 0; i < HG; ++i) qab[i] = *reinterpret_cast<const u32x4*>(qabl + i * D + 4 * c);

  float m = -INFINITY, l = 0.f, corr = 0.f;
  float acc[HG][4];
#pragma unroll
  for (int i = 0; i < HG; ++i)
#pragma unroll
    for (int j = 0; j < 4; ++j) acc[i][j] = 0.f;
  const bool b0 = c & 1, b1 = c & 2;
  const f32x4* scp = sc4 + (2 * wave + r) * HG + cls;

  auto step = [&](Unit& U, unsigned u, auto next) {
    constexpr bool NEXT = decltype(next)::value;
    const f32x4 sc = scp[u * (2 * WAVES * HG)];
    const unsigned cs0 = __builtin_amdgcn_perm(U.s[0], U.c[0], 0x05040100u);
    const unsigned cs1 = __builtin_amdgcn_perm(U.s[0], U.c[0], 0x07060302u);
    const unsigned cs2 = __builtin_amdgcn_perm(U.s[1], U.c[1], 0x05040100u);
    const unsigned cs3 = __builtin_amdgcn_perm(U.s[1], U.c[1], 0x07060302u);
    float x[HG];
    x[0] = qk_head<0>(U.k[0], cs0, cs1, cs2, cs3, qab[0], sc[1]);
    if constexpr (NEXT) { ld_t(U, u + 2); ld_k(U, u + 2, 0); __builtin_amdgcn_sched_barrier(0); }
    x[1] = qk_head<1>(U.k[1], cs0, cs1, cs2, cs3, qab[1], sc[1]);
    if constexpr (NEXT) { ld_k(U, u + 2, 1); __builtin_amdgcn_sched_barrier(0); }
    x[2] = qk_head<2>(U.k[2], cs0, cs1, cs2, cs3, qab[2], sc[1]);
    if constexpr (NEXT) { ld_k(U, u + 2, 2); __builtin_amdgcn_sched_barrier(0); }
    x[3] = qk_head<3>(U.k[3], cs0, cs1, cs2, cs3, qab[3], sc[1]);
    if constexpr (NEXT) { ld_k(U, u + 2, 3); __builtin_amdgcn_sched_barrier(0); }
    const float e0 = x[0] + dpp<XOR1>(x[0]), e1 = x[1] + dpp<XOR1>(x[1]);
    const float e2 = x[2] + dpp<XOR1>(x[2]), e3 = x[3] + dpp<XOR1>(x[3]);
    const float y0 = b0 ? e1 : e0;
    const float y1 = b0 ? e3 : e2;
    const float g0 = y0 + dpp<XOR2>(y0), g1 = y1 + dpp<XOR2>(y1);
    float z = b1 ? g1 : g0;
    z += dpp<ROR4>(z);
    z += dpp<ROR8>(z);
    z = x16_sum(z);
    const float s = z * sc[0];
    const float mn = fmaxf(m, s);
    const float alpha = __builtin_amdgcn_exp2f(m - mn);
    const float p = __builtin_amdgcn_exp2f(s - mn);
    m = mn;
    l = fmaf(l, alpha, p);
    const float pv = p * sc[2];
    corr = fmaf(corr, alpha, pv * sc[3]);
    pv_head<0>(acc[0], U.v[0], alpha, pv);
    if constexpr (NEXT) { ld_v(U, u + 2, 0); __builtin_amdgcn_sched_barrier(0); }
    pv_head<1>(acc[1], U.v[1], alpha, pv);
    if constexpr (NEXT) { ld_v(U, u + 2, 1); __builtin_amdgcn_sched_barrier(0); }
    pv_head<2>(acc[2], U.v[2], alpha, pv);
    if constexpr (NEXT) { ld_v(U, u + 2, 2); __builtin_amdgcn_sched_barrier(0); }
    pv_head<3>(acc[3], U.v[3], alpha, pv);
    if constexpr (NEXT) { ld_v(U, u + 2, 3); __builtin_amdgcn_sched_barrier(0); }
  };

#pragma unroll 1
  for (unsigned u = 0; u < NUNIT - 2; u += 2) {
    step(UA, u, Yes{});
    step(UB, u + 1, Yes{});
  }
  step(UA, NUNIT - 2, No{});
  step(UB, NUNIT - 1, No{});

  {
    float* rec = mrg + (wave * 2 + r) * HG * REC;
#pragma unroll
    for (int i = 0; i < HG; ++i) *reinterpret_cast<f32x4*>(rec + i * REC + 4 + 4 * c) = f32x4{acc[i][0], acc[i][1], acc[i][2], acc[i][3]};
    if (c < HG) *reinterpret_cast<f32x4*>(rec + c * REC) = f32x4{m, l, corr, 0.f};
  }
  __syncthreads();
  {
    const int hh = wave;
    constexpr int NR = WAVES * 2;
    f32x4 ml[NR]; f32x2 av[NR];
#pragma unroll
    for (int w = 0; w < NR; ++w) {
      const float* rec = mrg + (w * HG + hh) * REC;
      ml[w] = *reinterpret_cast<const f32x4*>(rec);
      av[w] = *reinterpret_cast<const f32x2*>(rec + 4 + 2 * lane);
    }
    float M = ml[0][0];
#pragma unroll
    for (int w = 1; w < NR; ++w) M = fmaxf(M, ml[w][0]);
    float L = 0.f, C = 0.f, o0 = 0.f, o1 = 0.f;
#pragma unroll
    for (int w = 0; w < NR; ++w) {
      const float f = __builtin_amdgcn_exp2f(ml[w][0] - M);
      L = fmaf(f, ml[w][1], L);
      C = fmaf(f, ml[w][2], C);
      o0 = fmaf(f, av[w][0], o0);
      o1 = fmaf(f, av[w][1], o1);
    }
    o0 -= C; o1 -= C;
    const unsigned aoff = WS_ACC + ((((unsigned)(grp * HG + hh) * 4 + (lane >> 4)) * NSPLIT + split) * 256) + (lane & 15) * 16;
    __builtin_amdgcn_raw_buffer_store_b128(u32x4{__float_as_uint(o0), tag, __float_as_uint(o1), tag}, wrs, aoff, 0, AUX_SC1);
    if (lane == 0) {
      const unsigned moff = WS_ML + ((unsigned)(grp * HG + hh) * NSPLIT + split) * 16;
      __builtin_amdgcn_raw_buffer_store_b128(u32x4{__float_as_uint(M), tag, __float_as_uint(L), tag}, wrs, moff, 0, AUX_SC1);
    }
  }
  if (wave != 0) return;
  {
    const int hh = split >> 2, sl = split & 3;
    const unsigned abase = WS_ACC + (((unsigned)(grp * HG + hh) * 4 + sl) * NSPLIT) * 256 + lane * 16;
    const unsigned mbase = WS_ML + ((unsigned)(grp * HG + hh) * NSPLIT + (lane >> 4)) * 16;
    u32x4 av[4], mv[4];
    bool ok = false;
    for (unsigned spins = 0; spins < (1u << 18); ++spins) {
      asm volatile("" ::: "memory");
#pragma unroll
      for (int n = 0; n < 4; ++n) {
        av[n] = __builtin_amdgcn_raw_buffer_load_b128(wrs, abase + n * 1024, 0, AUX_SC1);
        mv[n] = __builtin_amdgcn_raw_buffer_load_b128(wrs, mbase + n * 64, 0, AUX_SC1);
      }
      unsigned bad = 0;
#pragma unroll
      for (int n = 0; n < 4; ++n) bad |= (av[n][1] ^ tag) | (av[n][3] ^ tag) | (mv[n][1] ^ tag) | (mv[n][3] ^ tag);
      if (__all(bad == 0)) { ok = true; break; }
      __builtin_amdgcn_s_sleep(1);
    }
    float M = -INFINITY;
#pragma unroll
    for (int n = 0; n < 4; ++n) M = fmaxf(M, __uint_as_float(mv[n][0]));
    M = x32_max(x16_max(M));
    float L = 0.f, o0 = 0.f, o1 = 0.f;
#pragma unroll
    for (int n = 0; n < 4; ++n) {
      const float f = __builtin_amdgcn_exp2f(__uint_as_float(mv[n][0]) - M);
      L = fmaf(f, __uint_as_float(mv[n][2]), L);
      o0 = fmaf(f, __uint_as_float(av[n][0]), o0);
      o1 = fmaf(f, __uint_as_float(av[n][2]), o1);
    }
    L = x32_sum(x16_sum(L));
    o0 = x32_sum(x16_sum(o0));
    o1 = x32_sum(x16_sum(o1));
    const float inv = ok ? 1.f / L : __uint_as_float(0x7fc00000u);
    if (lane < 16) *reinterpret_cast<unsigned*>(a.out + (bh0 + hh) * D + sl * 32 + 2 * lane) = packbf(o0 * inv, o1 * inv);
    if (split == 0 && lane == 0 && ok) __builtin_amdgcn_raw_buffer_store_b32(tag, wrs, WS_EPOCH + grp * 256, 0, AUX_SC1);
  }
}

}

extern "C" void kernel_launch(void* const* d_in, const int* in_sizes, int n_in,
                              void* d_out, int out_size, void* d_ws, size_t ws_size,
                              hipStream_t stream) {
  Args a;
  a.q = (const unsigned short*)d_in[0];
  a.qk = (const int*)d_in[1];
  a.ksc = (const unsigned short*)d_in[2];
  a.kzp = (const unsigned short*)d_in[3];
  a.qv = (const int*)d_in[4];
  a.vsc = (const unsigned short*)d_in[5];
  a.vzp = (const unsigned short*)d_in[6];
  a.qcos = (const unsigned short*)d_in[7];
  a.qsin = (const unsigned short*)d_in[8];
  a.kcos = (const unsigned short*)d_in[9];
  a.ksin = (const unsigned short*)d_in[10];
  a.ws = (unsigned char*)d_ws;
  a.out = (__hip_bfloat16*)d_out;
  attn_decode<<<NWG, NTHR, 0, stream>>>(a);
}
```
